```python
import torch

def _softmax(x, axis=-1):
    return torch.softmax(x, dim=axis)
def _gelu(x):
    return torch.nn.functional.gelu(x, approximate="tanh")
def _silu(x):
    return torch.nn.functional.silu(x)
def _sigmoid(x):
    return torch.sigmoid(x)
def _layernorm(x, axis=-1, eps=1e-5):
    m = x.mean(dim=axis, keepdim=True)
    v = x.var(dim=axis, keepdim=True, unbiased=False)
    return (x - m) / torch.sqrt(v + eps)
def _scatter_add(tgt, idx, src, axis=0):
    return tgt.clone().index_add_(axis, idx, src)
def _segment_sum(src, seg, n):
    out = torch.zeros((n,) + src.shape[1:], dtype=src.dtype)
    return out.index_add_(0, seg, src)
def _pad(x, pw):
    flat = tuple(v for pair in reversed(pw) for v in pair)
    return torch.nn.functional.pad(x, flat)

kernel_name = 'parallel_sum__mha_block_cached__geglu_ffn__decode'

def setup_inputs():

    _w = (lambda a: (lambda t: t.to(torch.bfloat16) if t.is_floating_point() else t)(torch.as_tensor(a)))
    def _A_setup():
        B, Sq, D = 2, 1, 512
        H, Sk, Dh = 32, 4096, 128
        return {
            "x": _w((lambda *s: torch.randn(*s, dtype=torch.bfloat16) * 0.05)(B, Sq, D)),
            "k_cache": _w((lambda *s: torch.randn(*s, dtype=torch.bfloat16) * 3.2)(B, H, Sk, Dh)),
            "v_cache": _w((lambda *s: torch.randn(*s, dtype=torch.bfloat16) * 1.6)(B, H, Sk, Dh)),
            "wq": _w((lambda *s: torch.randn(*s, dtype=torch.bfloat16) * 3.2)(D, H * Dh) * (D ** -0.5)),
            "wo": _w((lambda *s: torch.randn(*s, dtype=torch.bfloat16) * 1.6)(H * Dh, D) * ((H * Dh) ** -0.5)),
        }
    _A_in = _A_setup()

    def _B_setup():
        B, S, D, F = 2, 1, 512, 1376
        return {
            "x": _w((lambda *s: torch.randn(*s, dtype=torch.bfloat16) * 0.05)(B, S, D)),
            "w_gate": _w((lambda *s: torch.randn(*s, dtype=torch.bfloat16) * 0.05)(D, F)),
            "w_up": _w((lambda *s: torch.randn(*s, dtype=torch.bfloat16) * 0.05)(D, F)),
            "w_down": _w((lambda *s: torch.randn(*s, dtype=torch.bfloat16) * 0.05)(F, D)),
        }
    _B_in = _B_setup()

    return {
        "A_x": _A_in["x"],
        "A_k_cache": _A_in["k_cache"],
        "A_v_cache": _A_in["v_cache"],
        "A_wq": _A_in["wq"],
        "A_wo": _A_in["wo"],
        "B_x": _B_in["x"],
        "B_w_gate": _B_in["w_gate"],
        "B_w_up": _B_in["w_up"],
        "B_w_down": _B_in["w_down"],
    }

def reference(A_x, A_k_cache, A_v_cache, A_wq, A_wo, B_x, B_w_gate, B_w_up, B_w_down):
    def _A_ref(x, k_cache, v_cache, wq, wo):
        B, Sq, D = x.shape
        H, Dh = 32, 128
        q = (lambda x, *d: x.permute(*d))((x @ wq).reshape(B, Sq, H, Dh), 0, 2, 1, 3)
        scores = torch.einsum("bhqd,bhkd->bhqk", q, k_cache) * (Dh ** -0.5)
        p = _softmax(scores, axis=-1)
        o = torch.einsum("bhqk,bhkd->bhqd", p, v_cache)
        return (lambda x, *d: x.permute(*d))(o, 0, 2, 1, 3).reshape(B, Sq, H * Dh) @ wo

    def _B_ref(x, w_gate, w_up, w_down):
        return (_gelu(x @ w_gate) * (x @ w_up)) @ w_down

    x = A_x
    xn = _layernorm(x, axis=-1)
    return x + _A_ref(xn, A_k_cache, A_v_cache, A_wq, A_wo) + _B_ref(xn, B_w_gate, B_w_up, B_w_down)
```

```cpp
#include <hip/hip_runtime.h>
#include <stdint.h>
#include <utility>

#define CODE_LINES 115
#define WLIM 40
#define QPOS 14
#define WD_POS 22
#define KM 16
#define TDELAY 5
#define APACE0 29
#define ATAU 450
#define AEND 19650
#define STAGW 56
#define YPOLL 125

namespace {
typedef unsigned u32x8 __attribute__((ext_vector_type(8)));
typedef unsigned u32x4 __attribute__((ext_vector_type(4)));
typedef unsigned u32x2 __attribute__((ext_vector_type(2)));
typedef float f32x4 __attribute__((ext_vector_type(4)));
typedef float f32x2 __attribute__((ext_vector_type(2)));
typedef __bf16 bf16x16 __attribute__((ext_vector_type(16)));
typedef __bf16 bf16x8 __attribute__((ext_vector_type(8)));
typedef __bf16 bf16x2 __attribute__((ext_vector_type(2)));
typedef short s16x4 __attribute__((ext_vector_type(4)));
typedef unsigned short u16;
typedef unsigned long long u64;
typedef __amdgpu_buffer_rsrc_t rsrc_t;

#define INL __attribute__((always_inline))
template <int I> struct IC { static constexpr int v = I; };
template <class F, int... Is> __device__ __forceinline__ void sfor_impl(F&& f, std::integer_sequence<int, Is...>) { (f(IC<Is>{}), ...); }
template <int N, class F> __device__ __forceinline__ void sfor(F&& f) { sfor_impl(f, std::make_integer_sequence<int, N>{}); }

constexpr int NT = 2, SC1 = 16;
constexpr int FF = 1376, FROW = 1440;
constexpr unsigned WS_EPOCH = 0;
constexpr unsigned WS_ML = 4096;
constexpr unsigned WS_TM = WS_ML + 64 * 128;
constexpr unsigned WS_QX = WS_TM + 2048;
constexpr unsigned WS_FX = WS_QX + 32 * 8 * 1024;
constexpr unsigned WS_Y = 1u << 20;
static_assert(WS_FX + 2 * 2 * 16 * FROW * 4 <= WS_Y, "ws map");
constexpr unsigned WS_END = WS_Y + 2 * 128 * 2048;
constexpr unsigned OOB = 0x7ff00000u;
constexpr int SPIN_CAP = 1 << 18;
constexpr int L_PL = 0, L_XN = 8192, L_Q = L_XN + 2048, L_H = L_Q + 256, L_ML = L_H + 32, L_MR = L_ML + 64, L_F = L_MR + 16,
              F_PL = L_F, F_Q = L_F + 4, F_ML = L_F + 8, F_MR = L_F + 12, F_KM = L_F + 16, F_XN = L_F + 20, L_T2 = L_F + 28,
              L_TAG = L_F + 32, L_TB = L_F + 44, L_RL = L_F + 80, L_OL = L_RL + 512, OL_STR = 144, L_END = L_OL + 16 * OL_STR;

enum { T_P, T_PF, T_K, T_V, T_WO, T_WD };
struct Item { int t, i; };
struct Act { int issue; int t, i; int k0, k1; };
constexpr int weight(int t) { return (t == T_WO || t == T_WD) ? 4 : 8; }
struct Sched {
  Act a[240]; int n;
  constexpr Sched() : a{}, n(0) {
    Item it[120] = {}; int ni = 0;
    for (int i = 0; i < 4; ++i) it[ni++] = Item{T_P, i};
    for (int i = 0; i < 3; ++i) it[ni++] = Item{T_PF, i};
    int wo = 0;
    for (int k = 0; k < 64; ++k) {
      it[ni++] = k < 32 ? Item{T_K, k} : Item{T_V, k - 32};
      if (k >= WD_POS && k < WD_POS + 4) it[ni++] = Item{T_WD, k - WD_POS};
      while (wo < 32 && k >= 3 && wo * 56 < (k - 2) * 32) it[ni++] = Item{T_WO, wo++};
    }
    int head = 0, fl = 0, kdone = 0, karr = 0;
    for (int j = 0; j <= ni; ++j) {
      while (head < j && (j == ni || fl + weight(it[j].t) > (head < 4 ? 48 : WLIM))) {
        Item r = it[head++]; fl -= weight(r.t);
        if (r.t == T_K) karr = r.i + 1;
        int k0 = kdone;
        if (karr > QPOS) kdone = (karr == 32 || kdone + 5 >= karr) ? karr : kdone + 5;
        a[n++] = Act{0, r.t, r.i, k0, kdone};
      }
      if (j < ni) { a[n++] = Act{1, it[j].t, it[j].i, 0, 0}; fl += weight(it[j].t); }
    }
  }
};

static_assert(WD_POS > QPOS, "h is written with q");

__device__ __forceinline__ float bflo(unsigned u) { return __uint_as_float(u << 16); }
__device__ __forceinline__ float bfhi(unsigned u) { return __uint_as_float(u & 0xffff0000u); }
__device__ __forceinline__ unsigned pkbf(float lo, float hi) { f32x2 f = {lo, hi}; bf16x2 h = __builtin_convertvector(f, bf16x2); return __builtin_bit_cast(unsigned, h); }
__device__ __forceinline__ float rbf(float f) { return bflo(pkbf(f, f)); }
__device__ __forceinline__ float dot2(unsigned a, unsigned b, float c) { return __builtin_amdgcn_fdot2_f32_bf16(__builtin_bit_cast(bf16x2, a), __builtin_bit_cast(bf16x2, b), c, false); }
template <int CTRL> __device__ __forceinline__ unsigned dppu(unsigned x) { return __builtin_amdgcn_update_dpp(0u, x, CTRL, 0xf, 0xf, true); }
template <int CTRL> __device__ __forceinline__ float dppf(float x) { return __uint_as_float(dppu<CTRL>(__float_as_uint(x))); }
constexpr int XOR1 = 0xB1, XOR2 = 0x4E, HMIR = 0x141, ROR8 = 0x128, ROR4 = 0x124, BCAST = 0x150;
__device__ __forceinline__ float row_sum(float x) { x += dppf<XOR1>(x); x += dppf<XOR2>(x); x += dppf<HMIR>(x); x += dppf<ROR8>(x); return x; }
__device__ __forceinline__ float row_max(float x) { x = fmaxf(x, dppf<XOR1>(x)); x = fmaxf(x, dppf<XOR2>(x)); x = fmaxf(x, dppf<HMIR>(x)); x = fmaxf(x, dppf<ROR8>(x)); return x; }
__device__ __forceinline__ float sw16(float a, float b) { auto s = __builtin_amdgcn_permlane16_swap(__float_as_uint(a), __float_as_uint(b), false, false); return __uint_as_float(s[0]) + __uint_as_float(s[1]); }
__device__ __forceinline__ float sw32(float a, float b) { auto s = __builtin_amdgcn_permlane32_swap(__float_as_uint(a), __float_as_uint(b), false, false); return __uint_as_float(s[0]) + __uint_as_float(s[1]); }
__device__ __forceinline__ float x16_max(float x) { auto s = __builtin_amdgcn_permlane16_swap(__float_as_uint(x), __float_as_uint(x), false, false); return fmaxf(__uint_as_float(s[0]), __uint_as_float(s[1])); }
__device__ __forceinline__ float x32_max(float x) { auto s = __builtin_amdgcn_permlane32_swap(__float_as_uint(x), __float_as_uint(x), false, false); return fmaxf(__uint_as_float(s[0]), __uint_as_float(s[1])); }
__device__ __forceinline__ float wave_sum(float x) { x = row_sum(x); x = sw16(x, x); return sw32(x, x); }
__device__ __forceinline__ float wave_max(float x) { return x32_max(x16_max(row_max(x))); }
__device__ __forceinline__ float tr4(f32x4 z) { return sw16(sw32(z.x, z.z), sw32(z.y, z.w)); }
__device__ __forceinline__ f32x4 mfma32(u32x4 a, u32x4 b, f32x4 c) { return __builtin_amdgcn_mfma_f32_16x16x32_bf16(__builtin_bit_cast(bf16x8, a), __builtin_bit_cast(bf16x8, b), c, 0, 0, 0); }
__device__ __forceinline__ f32x4 mfma16(u32x2 a, u32x2 b, f32x4 c) { return __builtin_amdgcn_mfma_f32_16x16x16bf16_1k(__builtin_bit_cast(s16x4, a), __builtin_bit_cast(s16x4, b), c, 0, 0, 0); }
__device__ __forceinline__ f32x4 smfma64(u32x4 a, u32x8 b, f32x4 c, int idx) { return __builtin_amdgcn_smfmac_f32_16x16x64_bf16(__builtin_bit_cast(bf16x8, a), __builtin_bit_cast(bf16x16, b), c, idx, 0, 0); }
__device__ __forceinline__ u32x4 ld16(rsrc_t r, unsigned v, unsigned s, int aux) { return aux == NT ? __builtin_amdgcn_raw_buffer_load_b128(r, v, s, NT) : aux == SC1 ? __builtin_amdgcn_raw_buffer_load_b128(r, v, s, SC1) : __builtin_amdgcn_raw_buffer_load_b128(r, v, s, 0); }
__device__ __forceinline__ void wg_barrier() { asm volatile("s_waitcnt lgkmcnt(0)\n\ts_barrier" ::: "memory"); }
__device__ __forceinline__ bool any(bool p) { return __builtin_amdgcn_ballot_w64(p) != 0; }
__device__ __forceinline__ unsigned now() { return (unsigned)__builtin_amdgcn_s_memrealtime(); }
__device__ __forceinline__ unsigned tagged(float v, unsigned m, unsigned t) { return ((__float_as_uint(v) + ((m + 1) >> 1)) & ~m) | (t & m); }
__device__ __forceinline__ u32x4 tagged4(f32x4 v, unsigned m, unsigned t) { return u32x4{tagged(v.x, m, t), tagged(v.y, m, t), tagged(v.z, m, t), tagged(v.w, m, t)}; }
__device__ __forceinline__ unsigned mism(u32x4 v, unsigned t) { return ((v.x ^ t) | (v.y ^ t)) | ((v.z ^ t) | (v.w ^ t)); }
__device__ __forceinline__ f32x4 untag(u32x4 v, unsigned m) { return __builtin_bit_cast(f32x4, v & ~m); }
#define CLOB() asm volatile("" ::: "memory")
#define TOUCH(x) asm volatile("" :: "v"(x) : "memory")
#define PIN(x) asm volatile("" : "+v"(x))
#define FENCE() __builtin_amdgcn_sched_barrier(0)
#define LDSW(off) (*(volatile __attribute__((address_space(3))) unsigned*)(lds + (off)))
constexpr int brev4(int t) { return ((t & 1) << 3) | ((t & 2) << 1) | ((t & 4) >> 1) | ((t & 8) >> 3); }
constexpr float LOG2E = 1.4426950408889634f;
__device__ __forceinline__ void lds_wait(unsigned char* lds, int off, unsigned want) {
  int spins = 0;
  while (LDSW(off) != want && spins < (SPIN_CAP << 4)) { ++spins; __builtin_amdgcn_s_sleep(1); }
  CLOB();
}
__device__ __forceinline__ void lds_add(unsigned char* lds, int off, unsigned v) { CLOB(); __hip_atomic_fetch_add((unsigned*)(lds + off), v, __ATOMIC_RELAXED, __HIP_MEMORY_SCOPE_WORKGROUP); CLOB(); }
template <int DUE> __device__ __forceinline__ void pace(unsigned vzero) {
  unsigned tv;
  asm volatile("ds_read_b32 %0, %1 offset:%c2\n"
               "P_%=:\n\ts_memrealtime vcc\n\ts_waitcnt lgkmcnt(0)\n\tv_readfirstlane_b32 vcc_hi, %0\n\ts_sub_i32 vcc_lo, vcc_lo, vcc_hi\n\t"
               "s_cmpk_lt_i32 vcc_lo, %c3\n\ts_cbranch_scc1 P_%="
               : "=&v"(tv) : "v"(vzero), "i"(L_TB), "i"(DUE) : "vcc", "scc", "memory");
}

enum { A_X, A_WQ, A_WG, A_WU, A_K, A_WS, A_V, A_WO, A_WD, A_OUT };
#define DESC(n) const void* p##n, unsigned n##n, unsigned f##n
__global__ void __launch_bounds__(576, 3)
fused_decode(DESC(x), DESC(wq), DESC(wg), DESC(wu), DESC(k), DESC(ws), DESC(v), DESC(wo), DESC(wd), DESC(out)) {
  __shared__ __attribute__((aligned(16))) unsigned char lds[L_END];
  const auto* ka = (const __attribute__((address_space(4))) rsrc_t*)__builtin_amdgcn_kernarg_segment_ptr();
  int tid = threadIdx.x, bid = blockIdx.x;
  rsrc_t rX, rP, rWO, rWS; u64 t0; int wv, h, par, span, ps1, ps2, ps3; unsigned poff, pfoff, ep, cpf; u32x4 x0, x1;
  { u64 pc; int t1, t2, t3, t4;
    asm volatile(
      "s_load_dwordx4 %[rX], %[ka], 0x0\n\t"
      "s_load_dwordx4 %[rWO], %[ka], 0x70\n\t"
      "s_load_dwordx4 %[rWS], %[ka], 0x50\n\t"
      "s_memrealtime %[t0]\n\t"
      "v_readfirstlane_b32 %[wv], %[tid]\n\t"
      "s_and_b32 %[t1], %[bid], 7\n\t"
      "s_lshr_b32 %[t2], %[bid], 6\n\t"
      "s_lshl2_add_u32 %[h], %[t1], %[t2]\n\t"
      "s_lshr_b32 %[wv], %[wv], 6\n\t"
      "s_cbranch_scc1 S_%=\n\t"
      "s_setprio 3\n\t"
      "s_getpc_b64 %[pc]\n\t"
      "s_lshr_b32 %[t1], %[bid], 3\n\t"
      "v_lshl_add_u32 %[poff], %[t1], 2, %[tid]\n\t"
      "v_min_u32 %[poff], %c[cl], %[poff]\n\t"
      "v_lshlrev_b32 %[poff], 7, %[poff]\n\t"
      "s_mov_b64 exec, 15\n\t"
      "global_load_dword %[cpf], %[poff], %[pc]\n\t"
      "s_mov_b64 exec, -1\n\t"
      "v_mov_b32 %[ep], 0\n\t"
      "v_lshlrev_b32 %[pfoff], 2, %[tid]\n\t"
      "ds_write_b32 %[pfoff], %[ep] offset:%c[lf]\n\t"
      "v_lshlrev_b32 %[poff], 5, %[tid]\n\t"
      "s_waitcnt lgkmcnt(0)\n\t"
      "buffer_load_dwordx4 %[x0], %[poff], %[rX], 0 offen\n\t"
      "buffer_load_dwordx4 %[x1], %[poff], %[rX], 0 offen offset:16\n\t"
      "buffer_load_dword %[ep], off, %[rWS], 0\n\t"
      "s_barrier\n\t"
      "s_branch E_%=\n"
      "S_%=:\n\t"
      "s_cmp_lt_u32 %[wv], 5\n\t"
      "s_cbranch_scc1 Q_%=\n\t"
      "s_cmpk_ge_u32 %[bid], 176\n\t"
      "s_cselect_b32 %[ps3], 0x30, 0x20\n\t"
      "s_cselect_b32 %[ps1], 11, 0\n\t"
      "s_lshr_b32 %[t4], %[bid], 4\n\t"
      "s_sub_u32 %[t4], %[t4], %[ps1]\n\t"
      "s_bfe_u32 %[t3], %[bid], 0x20003\n\t"
      "s_lshl2_add_u32 %[t2], %[t2], %[t3]\n\t"
      "s_add_u32 %[t2], %[t2], 5\n\t"
      "s_bfe_u32 %[ps1], %[bid], 0x10005\n\t"
      "s_lshl1_add_u32 %[t1], %[t1], %[ps1]\n\t"
      "s_cmp_ge_u32 %[wv], 7\n\t"
      "s_cselect_b32 %[ps3], 0x30, %[ps3]\n\t"
      "s_cselect_b32 %[t4], %[t2], %[t4]\n\t"
      "s_cselect_b32 %[t1], %[t1], %[bid]\n\t"
      "s_load_dwordx4 %[rP], %[ka], %[ps3]\n\t"
      "s_and_b32 %[par], %[t1], 1\n\t"
      "s_and_b32 %[span], %[t1], 14\n\t"
      "s_andn2_b32 %[t1], 1, %[wv]\n\t"
      "s_or_b32 %[span], %[span], %[t1]\n\t"
      "s_lshl_b32 %[t4], %[t4], 2\n\t"
      "s_sub_u32 %[t4], %[t4], %[par]\n\t"
      "s_lshl_b32 %[t4], %[t4], 6\n\t"
      "s_movk_i32 %[t2], 2752\n\t"
      "s_branch J_%=\n"
      "Q_%=:\n\t"
      "s_load_dwordx4 %[rP], %[ka], 0x10\n\t"
      "s_bfe_u32 %[par], %[bid], 0x10003\n\t"
      "s_lshr_b32 %[t1], %[bid], 2\n\t"
      "s_and_b32 %[t1], %[t1], 12\n\t"
      "s_add_u32 %[span], %[t1], %[wv]\n\t"
      "s_sub_u32 %[span], %[span], 1\n\t"
      "s_lshl_b32 %[t4], %[h], 8\n\t"
      "s_movk_i32 %[t2], 8192\n"
      "J_%=:\n\t"
      "v_lshrrev_b32 %[poff], 1, %[tid]\n\t"
      "s_lshl_b32 %[t1], %[span], 5\n\t"
      "v_and_b32 %[pfoff], 15, %[tid]\n\t"
      "s_or_b32 %[t1], %[t1], %[par]\n\t"
      "v_lshl_add_u32 %[pfoff], %[pfoff], 4, %[t4]\n\t"
      "v_and_or_b32 %[poff], %[poff], 24, %[t1]\n\t"
      "s_bfe_u32 %[t1], %[bid], 0x30003\n\t"
      "v_cmp_gt_u32 vcc, %[t2], %[pfoff]\n\t"
      "s_lshl3_add_u32 %[t1], %[h], %[t1]\n\t"
      "v_mad_u32_u24 %[poff], %[poff], %[t2], %[pfoff]\n\t"
      "v_mov_b32 %[pfoff], 0x7ff00000\n\t"
      "s_lshl_b32 %[t1], %[t1], 14\n\t"
      "v_cndmask_b32 %[poff], %[pfoff], %[poff], vcc\n\t"
      "s_sub_u32 %[t1], %[t1], 0x400\n\t"
      "s_lshl_b32 %[ps1], %[t2], 1\n\t"
      "v_lshl_add_u32 %[pfoff], %[tid], 4, %[t1]\n\t"
      "s_lshl_b32 %[ps2], %[t2], 2\n\t"
      "s_waitcnt lgkmcnt(0)\n\t"
      "s_mul_i32 %[ps3], %[t2], 6\n"
      "E_%=:"
      : [rX] "=&s"(rX), [rP] "=&s"(rP), [rWO] "=&s"(rWO), [rWS] "=&s"(rWS), [t0] "=&s"(t0), [wv] "=&s"(wv), [h] "=&s"(h), [par] "=&s"(par), [span] "=&s"(span),
        [ps1] "=&s"(ps1), [ps2] "=&s"(ps2), [ps3] "=&s"(ps3), [poff] "=&v"(poff), [pfoff] "=&v"(pfoff), [cpf] "=&v"(cpf), [x0] "=&v"(x0), [x1] "=&v"(x1), [ep] "=&v"(ep),
        [pc] "=&s"(pc), [t1] "=&s"(t1), [t2] "=&s"(t2), [t3] "=&s"(t3), [t4] "=&s"(t4)
      : [ka] "s"(ka), [bid] "s"(bid), [tid] "v"(tid), [cl] "i"(CODE_LINES - 1), [lf] "i"(L_F)
      : "vcc", "scc", "memory");
  }
  asm("" : "+s"(rX)); asm("" : "+s"(rWO)); asm("" : "+s"(rP)); asm("" : "+s"(rWS)); asm("" : "+s"(wv)); asm("" : "+s"(h)); asm("" : "+s"(par)); asm("" : "+s"(span)); asm("" : "+s"(t0));
  static_assert(WS_EPOCH == 0 && 2 * FF == 2752 && OOB == 0x7ff00000u && A_WO == 7 && A_WS == 5 && L_F + 256 <= L_END, "constants of the block above");
#define INDICES   \
  asm volatile("" : "+s"(bid), "+s"(wv)); asm volatile("" : "+v"(tid)); \
  const int lane = tid & 63, g = lane >> 4, c = lane & 15, xcd = bid & 7, j = bid >> 3; \
  const int hq = j >> 3, b = (j >> 2) & 1, s = j & 3, i16 = hq * 4 + s; \
  const int dcnt = i16 < 3 ? 4 : i16 < 6 ? 8 : i16 < 12 ? 12 : 16, dstart = xcd * 172 + dcnt * i16 - (i16 < 3 ? 0 : i16 < 6 ? 12 : i16 < 12 ? 36 : 84)

  if (wv == 0) {
    INDICES;
    const unsigned tstart = (unsigned)t0;
    if (lane < 9) LDSW(L_TB + lane * 4) = tstart - (1u << 30);
    FENCE();
    {
      float e[16];
      asm volatile("s_waitcnt vmcnt(1)" : "+v"(x0), "+v"(x1) :: "memory");
      sfor<4>([&](auto ec) INL { constexpr int k = decltype(ec)::v; const unsigned u0 = x0[k], u1 = x1[k];
        e[2 * k] = bflo(u0); e[2 * k + 1] = bfhi(u0); e[8 + 2 * k] = bflo(u1); e[9 + 2 * k] = bfhi(u1); });
      float a = (((e[0] + e[1]) + (e[2] + e[3])) + ((e[4] + e[5]) + (e[6] + e[7]))) + (((e[8] + e[9]) + (e[10] + e[11])) + ((e[12] + e[13]) + (e[14] + e[15])));
      a = row_sum(a); a = sw16(a, a) * (1.f / 512.f);
      float v0 = 0.f, v1 = 0.f;
      sfor<8>([&](auto ec) INL { constexpr int k = decltype(ec)::v; const float d0 = e[k] - a, d1 = e[8 + k] - a; v0 = fmaf(d0, d0, v0); v1 = fmaf(d1, d1, v1); });
      float v = row_sum(v0 + v1); v = sw16(v, v) * (1.f / 512.f);
      const float mean = rbf(a), rden = __builtin_amdgcn_rcpf(rbf(__builtin_amdgcn_sqrtf(rbf(rbf(v) + 1e-5f))));
      u32x4 n0, n1;
      sfor<4>([&](auto ec) INL { constexpr int k = decltype(ec)::v;
        const unsigned d0 = pkbf(e[2 * k] - mean, e[2 * k + 1] - mean), d1 = pkbf(e[8 + 2 * k] - mean, e[9 + 2 * k] - mean);
        n0[k] = pkbf(bflo(d0) * rden, bfhi(d0) * rden); n1[k] = pkbf(bflo(d1) * rden, bfhi(d1) * rden); });
      *(u32x4*)(lds + L_XN + lane * 32) = n0; *(u32x4*)(lds + L_XN + lane * 32 + 16) = n1;
    }
    CLOB(); LDSW(F_XN) = 1u;
    FENCE();
    asm volatile("s_waitcnt vmcnt(0)" : "+v"(ep), "+v"(cpf) :: "memory");
    const rsrc_t rV = ka[A_V], rOut = ka[A_OUT];
    unsigned ooff = (lane < 16 && s == 0) ? lane * 2 : OOB;
    const unsigned xr = __builtin_amdgcn_raw_buffer_load_b32(rX, ooff & ~2u, b * 1024 + h * 32, 0);
    const unsigned tl0 = __builtin_amdgcn_raw_buffer_load_b32(rV, 0, ((b * 32 + h) * 4 + s) << 18, NT), tl1 = __builtin_amdgcn_raw_buffer_load_b32(rOut, ooff & ~2u, b * 1024 + h * 32, 0);
    const unsigned tag = ((ep * 747796405u + 2891336453u) & ~0xfu) | ((ep + 1u) & 0xfu);
    LDSW(L_TAG) = tag;
    const int mA = bid >= 176, cA = (bid >> 4) - 11 * mA, sA = bid & 15, cB = 5 + i16, sB = 2 * xcd + b;
    const unsigned dQ = WS_QX + (h * 8 + (j & 7)) * 1024, dA = WS_FX + (mA * 16 + sA) * (2 * FROW * 4) + (cA * 128 + 32 - 32 * (sA & 1)) * 4,
                   dB = WS_FX + (16 + sB) * (2 * FROW * 4) + (cB * 128 + 32 - 32 * (sB & 1)) * 4;
    unsigned fvo = (lane & 31) * 16 + (lane >> 5) * (FROW * 4), qo = (lane >> 5) * 1024 + (lane & 31) * 16;
    const bool fon = 4 * (lane & 3) < dcnt;
    unsigned fo = fon ? (lane >> 2) * (2 * FROW * 4) + (lane & 3) * 16 : OOB, fmask = fon ? 0xffu : 0u;
    const unsigned fbase = WS_FX + b * (FROW * 4) + (32 + dstart) * 4;
    PIN(fvo); PIN(qo); PIN(fo); PIN(fmask);
    lds_wait(lds, F_PL, 8);
    { const unsigned char* src = lds + L_PL + lane * 16;
      const f32x4 vq = (*(const f32x4*)src + *(const f32x4*)(src + 1024)) + (*(const f32x4*)(src + 2048) + *(const f32x4*)(src + 3072));
      const f32x4 vA = *(const f32x4*)(src + 4096) + *(const f32x4*)(src + 5120), vB = *(const f32x4*)(src + 6144) + *(const f32x4*)(src + 7168);
      __builtin_amdgcn_raw_buffer_store_b128(tagged4(vq, 3u, tag), rWS, lane * 16, dQ, SC1);
      __builtin_amdgcn_raw_buffer_store_b128(tagged4(vA, 0xffu, tag), rWS, fvo, dA, SC1);
      __builtin_amdgcn_raw_buffer_store_b128(tagged4(vB, 0xffu, tag), rWS, i16 < 6 ? fvo : OOB, dB, SC1);
    }
    asm volatile("s_waitcnt vmcnt(0)" ::: "memory");
    { u32x4 qp[4], fg, fu; int spins = 0;
      _Pragma("unroll 1") do {
        CLOB();
        sfor<4>([&](auto kc) INL { constexpr int k = decltype(kc)::v; qp[k] = ld16(rWS, qo, WS_QX + h * 8192 + b * 512 + k * 2048, SC1); });
        fg = ld16(rWS, fo, fbase, SC1); fu = ld16(rWS, fo, fbase + 16 * 2 * FROW * 4, SC1);
      } while (any((((mism(qp[0], tag) | mism(qp[1], tag) | mism(qp[2], tag) | mism(qp[3], tag)) & 3u) | ((mism(fg, tag) | mism(fu, tag)) & fmask)) != 0) && ++spins < SPIN_CAP);
      const f32x4 qs = (untag(qp[0], 3u) + untag(qp[1], 3u)) + (untag(qp[2], 3u) + untag(qp[3], 3u));
      *(u32x2*)(lds + L_Q + (lane & 31) * 8) = u32x2{pkbf(sw32(qs.x, qs.x), sw32(qs.y, qs.y)), pkbf(sw32(qs.z, qs.z), sw32(qs.w, qs.w))};
      float gv = tr4(untag(fg, 0xffu)), uv = tr4(untag(fu, 0xffu));
      gv += dppf<ROR8>(gv); uv += dppf<ROR8>(uv); gv += dppf<ROR4>(gv); uv += dppf<ROR4>(uv);
      gv = rbf(gv); uv = rbf(uv);
      const float z = 0.7978845608028654f * (gv + 0.044715f * gv * gv * gv);
      const float ge = rbf(gv * __builtin_amdgcn_rcpf(1.f + __builtin_amdgcn_exp2f(-2.f * LOG2E * z)));
      if (c < 4) *(u16*)(lds + L_H + (4 * c + g) * 2) = (u16)pkbf(ge * uv, 0.f);
      CLOB(); LDSW(F_Q) = 1u;
    }
    TOUCH(tl0); TOUCH(tl1); TOUCH(xr);
    { lds_wait(lds, F_KM, 8);
      const unsigned tK = tstart + ((LDSW(L_T2) - 8u * tstart) >> 3);
      __builtin_amdgcn_raw_buffer_store_b64(u32x2{tK, tag}, rWS, (lane == 0 && s == hq && b == (hq & 1)) ? 0u : OOB, WS_TM + h * 8, SC1);
      for (int k = 0; k < TDELAY; ++k) __builtin_amdgcn_s_sleep(16);
      const int dn = (int)(now() - tK);
      const u32x2 ta = __builtin_amdgcn_raw_buffer_load_b64(rWS, (lane & 31) * 8, WS_TM, SC1);
      const float mt = wave_sum((float)(ta.y == tag ? (int)(ta.x - tK) : dn)) * (1.f / 64.f);
      if (lane < 9) LDSW(L_TB + lane * 4) = tK + (int)fminf(mt, 400.f) + (lane ? ((2 * lane - 9) * STAGW) >> 5 : 0);
    }
    { const unsigned mb = WS_ML + (b * 32 + h) * 128;
      lds_wait(lds, F_ML, 8);
      const f32x2 ml = *(const f32x2*)(lds + L_ML + (lane & 7) * 8);
      float m = fmaxf(ml.x, dppf<XOR1>(ml.x)); m = fmaxf(m, dppf<XOR2>(m)); m = fmaxf(m, dppf<HMIR>(m));
      float l = ml.y * __builtin_amdgcn_exp2f((ml.x - m) * LOG2E); l += dppf<XOR1>(l); l += dppf<XOR2>(l); l += dppf<HMIR>(l);
      __builtin_amdgcn_raw_buffer_store_b128(u32x4{__float_as_uint(m), tag, __float_as_uint(l), tag}, rWS, lane == 0 ? 0u : OOB, mb + s * 16, SC1);
      __builtin_amdgcn_s_sleep(127);
      u32x4 mp; int spins = 0;
      _Pragma("unroll 1") do {
        __builtin_amdgcn_s_sleep(8); CLOB();
        mp = ld16(rWS, (lane & 3) * 16, mb, SC1);
      } while (any(mp.y != tag || mp.w != tag) && ++spins < SPIN_CAP);
      const float mj = __uint_as_float(mp.x), lj = __uint_as_float(mp.z);
      float M = fmaxf(mj, dppf<XOR1>(mj)); M = fmaxf(M, dppf<XOR2>(M));
      float L = lj * __builtin_amdgcn_exp2f((mj - M) * LOG2E); L += dppf<XOR1>(L); L += dppf<XOR2>(L);
      *(f32x2*)(lds + L_MR) = f32x2{M, __builtin_amdgcn_rcpf(L)};
      CLOB(); LDSW(F_MR) = 1u;
    }
    unsigned rrd = L_RL + c * 32; float xf = (lane & 1) ? bfhi(xr) : bflo(xr);
    PIN(rrd); PIN(xf); PIN(ooff);
    wg_barrier();
    if (s != 0) return;
    wg_barrier();
    const f32x4 r4 = *(const f32x4*)(lds + rrd) + *(const f32x4*)(lds + rrd + 16);
    __builtin_amdgcn_raw_buffer_store_b16((u16)pkbf(xf + ((r4.x + r4.y) + (r4.z + r4.w)), 0.f), rOut, ooff, b * 1024 + h * 32, 0);
    if (bid == 0) __builtin_amdgcn_raw_buffer_store_b32(tag, rWS, lane == 0 ? 0u : OOB, WS_EPOCH, 0);
    return;
  }

  u32x4 P[4], PF[3], KV[64];
  P[0] = ld16(rP, poff, 0, NT); P[1] = ld16(rP, poff, ps1, NT); P[2] = ld16(rP, poff, ps2, NT); P[3] = ld16(rP, poff, ps3, NT);
  asm volatile("s_barrier" ::: "memory");
  PF[0] = ld16(rWO, pfoff, 0, 0); PF[1] = ld16(rWO, pfoff, 8192, 0);
  CLOB(); FENCE();
  const rsrc_t rK = ka[A_K], rV = ka[A_V], rWD = ka[A_WD];
  INDICES;
  const int w = wv - 1, rot = (bid << 12) & 0x38000;
  unsigned kvoff = (((b * 32 + h) * 4 + s) << 18) + (tid - 64) * 16; PIN(kvoff);
  u32x4 sel, q;
  u32x2 WO[32], WD[4];
  u32x8 BO[8];
  unsigned wooff = 0, wdoff = 0, owr = 0, ord = 0, yst = 0, yld = 0, rwr = 0, ytag = 0, vzero = 0, vw4 = wv * 4;
  int sidx = 0;
  f32x4 oacc = {0.f, 0.f, 0.f, 0.f}, yacc[4];
  float t1 = 0.f, t2 = 0.f, t3 = 0.f, t4 = 0.f, sc[2] = {0.f, 0.f}, mw = 0.f, cw = 0.f;
  unsigned pp[2] = {0u, 0u};
  auto mkA = [&](unsigned src) INL -> u32x4 { u32x4 a; a.x = __builtin_amdgcn_perm(src, src, sel.x); a.y = __builtin_amdgcn_perm(src, src, sel.y);
                                              a.z = __builtin_amdgcn_perm(src, src, sel.z); a.w = __builtin_amdgcn_perm(src, src, sel.w); return a; };
  auto tile_off = [&](int t) INL { int r = rot; asm volatile("" : "+s"(r)); return (r + t * 8192) & 0x3ffff; };

  auto consumeK = [&](auto tc) INL {
    constexpr int t = decltype(tc)::v;
    const u32x4 k = KV[t];
    float d = dot2(k.x, q.x, 0.f); d = dot2(k.y, q.y, d); d = dot2(k.z, q.z, d); d = dot2(k.w, q.w, d);
    if constexpr ((t & 1) == 0) { t1 = d; PIN(t1); }
    else {
      { const float keep = (c & 8) ? d : t1, send = (c & 8) ? t1 : d; d = keep + dppf<ROR8>(send); }
      if constexpr ((t & 2) == 0) { t2 = d; PIN(t2); }
      else {
        { const float keep = (c & 4) ? d : t2, send = (c & 4) ? t2 : d; d = keep + dppf<HMIR>(send); }
        if constexpr ((t & 4) == 0) { t3 = d; PIN(t3); }
        else {
          { const float keep = (c & 2) ? d : t3, send = (c & 2) ? t3 : d; d = keep + dppf<XOR2>(send); }
          if constexpr ((t & 8) == 0) { t4 = d; PIN(t4); }
          else { const float keep = (c & 1) ? d : t4, send = (c & 1) ? t4 : d; sc[t >> 4] = keep + dppf<XOR1>(send); PIN(sc[t >> 4]); }
        }
      }
    }
    if constexpr (t == 31) {
      const float s0 = rbf(rbf(sc[0]) * 0.08838834764831845f), s1 = rbf(rbf(sc[1]) * 0.08838834764831845f);
      mw = wave_max(fmaxf(s0, s1));
      const float p0 = __builtin_amdgcn_exp2f((s0 - mw) * LOG2E), p1 = __builtin_amdgcn_exp2f((s1 - mw) * LOG2E);
      const float lw = wave_sum(p0 + p1);
      pp[0] = pkbf(p0, p0 - rbf(p0)); pp[1] = pkbf(p1, p1 - rbf(p1)); PIN(pp[0]); PIN(pp[1]);
      if (lane == 0) { *(f32x2*)(lds + L_ML + w * 8) = f32x2{mw, lw}; lds_add(lds, F_ML, 1u); }
    }
  };

  constexpr Sched S{};
  sfor<S.n>([&](auto nc) INL {
    constexpr Act A = S.a[decltype(nc)::v];
    constexpr int i = A.i;
    if constexpr (A.issue) {
      if constexpr (A.t == T_K || A.t == T_V) {
        constexpr int n = i + (A.t == T_V ? 32 : 0);
        if constexpr (n >= APACE0) { pace<(AEND - (63 - n) * ATAU) / 16>(vw4); FENCE(); }
        KV[n] = ld16(A.t == T_V ? rV : rK, kvoff, tile_off(i), NT);
      }
      else if constexpr (A.t == T_PF && i == 2) PF[2] = ld16(rWD, 2 * w < dcnt ? (tid - 64) * 16 : OOB, (dstart + b * (dcnt >> 1)) * 1024, 0);
      else if constexpr (A.t == T_WO) WO[i] = __builtin_amdgcn_raw_buffer_load_b64(rWO, wooff, ((i >> 3) * 32 + (i & 7)) * 1024, NT);
      else if constexpr (A.t == T_WD) WD[i] = __builtin_amdgcn_raw_buffer_load_b64(rWD, wdoff, i * 1024, NT);
    } else {
      if constexpr (A.t == T_P) {
        if constexpr (i == 0) {
          const unsigned sb = (c & 8) ? 0x0302u : 0x0100u, one = (c & 1) ? ((sb << 16) | 0x0c0cu) : (0x0c0c0000u | sb); const int v = (c & 7) >> 1;
          sel.x = v == 0 ? one : 0x0c0c0c0cu; sel.y = v == 1 ? one : 0x0c0c0c0cu; sel.z = v == 2 ? one : 0x0c0c0c0cu; sel.w = v == 3 ? one : 0x0c0c0c0cu;
          wooff = h * 131072 + g * 8192 + w * 128 + c * 8;
          wdoff = (4 * g < dcnt) ? (unsigned)(dstart * 1024 + g * 4096 + w * 128 + c * 8) : OOB;
          owr = L_OL + (w + (lane >= 32 ? 8 : 0)) * OL_STR + c * 8 + (g & 1) * 4;
          ord = L_OL + c * OL_STR + g * 16;
          sidx = (c & 8) ? 0xeeee : 0x4444;
          yst = g ? OOB : WS_Y + (b * 128 + h * 4 + s) * 2048 + w * 256 + c * 16;
          yld = WS_Y + b * 128 * 2048 + h * 64 + ((tid - 64) >> 2) * 2048 + (lane & 3) * 16;
          rwr = c < 4 ? L_RL + (4 * c + g) * 32 + w * 4 : L_PL + lane * 4;
          PIN(sel); PIN(wooff); PIN(wdoff); PIN(owr); PIN(ord); PIN(sidx); PIN(yst); PIN(yld); PIN(rwr); PIN(vzero); PIN(vw4);
        }
        TOUCH(P[i]);
        if constexpr (i == 3) {
          lds_wait(lds, F_XN, 1u);
          const u32x4 xa = *(const u32x4*)(lds + L_XN + (c >> 3) * 1024 + g * 16 + span * 64);
          f32x4 pacc = {0.f, 0.f, 0.f, 0.f};
          sfor<4>([&](auto ec) INL { constexpr int e = decltype(ec)::v; const unsigned u = xa[e];
            pacc = mfma32(mkA(__builtin_amdgcn_perm(u, u, par ? 0x03020302u : 0x01000100u)), P[e], pacc); });
          *(f32x4*)(lds + L_PL + w * 1024 + (g >> 1) * 512 + c * 32 + (g & 1) * 16) = pacc;
          if (lane == 0) lds_add(lds, F_PL, 1u);
        }
      } else if constexpr (A.t == T_PF) { TOUCH(PF[i]);
      } else if constexpr (A.t == T_K) {
        TOUCH(KV[i]);
        if constexpr (i == KM) { const unsigned t = now(); if (lane == 0) { lds_add(lds, L_T2, t); lds_add(lds, F_KM, 1u); } }
        if constexpr (i == QPOS) {
          lds_wait(lds, F_Q, 1u);
          q = *(const u32x4*)(lds + L_Q + c * 16);
          ytag = LDSW(L_TAG) & 0xfu;
          PIN(ytag);
        }
      } else if constexpr (A.t == T_WD) {
        TOUCH(WD[i]);
        if constexpr (i == 3) {
          u32x2 hA = *(const u32x2*)(lds + L_H + g * 8);
          if (c != 0) hA = u32x2{0u, 0u};
          sfor<4>([&](auto tc) INL { constexpr int t = decltype(tc)::v; constexpr unsigned ps = (t & 1) ? 0x07060302u : 0x05040100u;
            const unsigned w0 = WD[0][t >> 1], w1 = WD[1][t >> 1], w2 = WD[2][t >> 1], w3 = WD[3][t >> 1];
            const u32x2 bb = {__builtin_amdgcn_perm(w1, w0, ps), __builtin_amdgcn_perm(w3, w2, ps)};
            yacc[t] = mfma16(hA, bb, f32x4{0.f, 0.f, 0.f, 0.f}); PIN(yacc[t]); });
        }
      } else if constexpr (A.t == T_WO) {
        TOUCH(WO[i]);
        if constexpr ((i & 7) == 7) {
          constexpr int u = i >> 3;
          sfor<4>([&](auto tc) INL { constexpr int t = decltype(tc)::v; constexpr unsigned ps = (t & 1) ? 0x07060302u : 0x05040100u;
            sfor<4>([&](auto vc) INL { constexpr int v = decltype(vc)::v;
              const unsigned lo = WO[u * 8 + 2 * v][t >> 1], hi = WO[u * 8 + 2 * v + 1][t >> 1];
              BO[(u >> 1) * 4 + t][(u & 1) * 4 + v] = __builtin_amdgcn_perm(hi, lo, ps); });
            PIN(BO[(u >> 1) * 4 + t]); });
        }
      } else if constexpr (A.t == T_V) {
        oacc = mfma32(mkA(dppu<BCAST + brev4(i & 15)>(pp[i >> 4])), KV[32 + i], oacc); PIN(oacc);
        if constexpr (i == 27) {
          lds_wait(lds, F_MR, 1u);
          const f32x2 mr = *(const f32x2*)(lds + L_MR);
          cw = __builtin_amdgcn_exp2f((mw - mr.x) * LOG2E) * mr.y;
          PIN(cw);
        }
      }
      sfor<A.k1 - A.k0>([&](auto kc) INL { FENCE(); consumeK(IC<A.k0 + decltype(kc)::v>{}); });
    }
    FENCE();
  });

  *(unsigned*)(lds + owr) = pkbf(sw32(oacc.x, oacc.z) * cw, sw32(oacc.y, oacc.w) * cw);
  wg_barrier();
  { const u32x4 a0 = *(const u32x4*)(lds + ord), a1 = *(const u32x4*)(lds + ord + 64);
    FENCE();
    auto sum4 = [&](f32x4 u) INL { const f32x2 p = f32x2{u.x, u.y} + f32x2{u.z, u.w}; return p.x + p.y; };
    auto fin = [&](float y) INL { y = sw16(y, y); const unsigned t = ((__float_as_uint(y) + 8u) & ~0xfu) | ytag; return __builtin_amdgcn_permlane32_swap(t, t, false, false); };
    yacc[0] = smfma64(a0, BO[0], yacc[0], sidx); yacc[2] = smfma64(a0, BO[2], yacc[2], sidx); FENCE();
    yacc[0] = smfma64(a1, BO[4], yacc[0], sidx); yacc[2] = smfma64(a1, BO[6], yacc[2], sidx); FENCE();
    yacc[1] = smfma64(a0, BO[1], yacc[1], sidx); FENCE();
    float z0 = sum4(yacc[0]); PIN(z0); FENCE();
    yacc[3] = smfma64(a0, BO[3], yacc[3], sidx); FENCE();
    float z2 = sum4(yacc[2]); PIN(z2); FENCE();
    yacc[1] = smfma64(a1, BO[5], yacc[1], sidx); FENCE();
    float ya = sw32(z0, z2); PIN(ya); FENCE();
    yacc[3] = smfma64(a1, BO[7], yacc[3], sidx); FENCE();
    const auto sa = fin(ya);
    const auto sb = fin(sw32(sum4(yacc[1]), sum4(yacc[3])));
    __builtin_amdgcn_raw_buffer_store_b128(u32x4{sa[0], sb[0], sa[1], sb[1]}, rWS, yst, 0, SC1);
  }
  if (s != 0) return;
  { u32x4 y; int spins = SPIN_CAP;
    pace<AEND / 16 + YPOLL>(vzero);
    unsigned t0_, t1_;
    asm volatile("s_waitcnt vmcnt(0)\n"
                 "L_%=:\n\tbuffer_load_dwordx4 v[2:5], %[off], %[rs], 0 offen sc1\n\ts_waitcnt vmcnt(0)\n\t"
                 "v_bitop3_b32 %[t0], v2, v3, %[tag] bitop3:0x7e\n\tv_bitop3_b32 %[t1], v4, v5, %[tag] bitop3:0x7e\n\t"
                 "v_bitop3_b32 %[t0], %[t0], %[t1], 15 bitop3:0xa8\n\tv_cmp_ne_u32 vcc, 0, %[t0]\n\ts_cbranch_vccz D_%=\n\t"
                 "s_sub_u32 %[sp], %[sp], 1\n\ts_cbranch_scc0 L_%=\n"
                 "D_%=:"
                 : "=&{v[2:5]}"(y), [t0] "=&v"(t0_), [t1] "=&v"(t1_), [sp] "+s"(spins) : [off] "v"(yld), [rs] "s"(rWS), [tag] "v"(ytag) : "vcc", "scc", "memory");
    float v = tr4(untag(y, 0xfu)); v += dppf<ROR8>(v); v += dppf<ROR4>(v);
    *(float*)(lds + rwr) = v;
    wg_barrier();
  }
}
}

extern "C" void kernel_launch(void* const* d_in, const int* in_sizes, int n_in, void* d_out, int out_size, void* d_ws, size_t ws_size, hipStream_t stream) {
  const unsigned F = 0x00020000u;
  fused_decode<<<256, 576, 0, stream>>>(d_in[0], 2048u, F, d_in[3], 512u * 8192u, F, d_in[6], 512u * 2u * FF, F, d_in[7], 512u * 2u * FF, F, d_in[1], 64u << 20, F,
                                        d_ws, WS_END, F, d_in[2], 64u << 20, F, d_in[4], 4096u * 1024u, F, d_in[8], FF * 1024u, F, d_out, 2048u, F);
}
```

```cpp
#include <hip/hip_runtime.h>
#include <stdint.h>
#include <utility>

#define CODE_LINES 115
#define WLIM 40
#define QPOS 14
#define WD_POS 22
#define KM 16
#define TDELAY 5
#define APACE0 29
#define ATAU 450
#define AEND 19650
#define STAGW 56
#define SK 0
#define YPOLL 125

namespace {
typedef unsigned u32x8 __attribute__((ext_vector_type(8)));
typedef unsigned u32x4 __attribute__((ext_vector_type(4)));
typedef unsigned u32x2 __attribute__((ext_vector_type(2)));
typedef float f32x4 __attribute__((ext_vector_type(4)));
typedef float f32x2 __attribute__((ext_vector_type(2)));
typedef __bf16 bf16x16 __attribute__((ext_vector_type(16)));
typedef __bf16 bf16x8 __attribute__((ext_vector_type(8)));
typedef __bf16 bf16x2 __attribute__((ext_vector_type(2)));
typedef short s16x4 __attribute__((ext_vector_type(4)));
typedef unsigned short u16;
typedef unsigned long long u64;
typedef __amdgpu_buffer_rsrc_t rsrc_t;

#define INL __attribute__((always_inline))
template <int I> struct IC { static constexpr int v = I; };
template <class F, int... Is> __device__ __forceinline__ void sfor_impl(F&& f, std::integer_sequence<int, Is...>) { (f(IC<Is>{}), ...); }
template <int N, class F> __device__ __forceinline__ void sfor(F&& f) { sfor_impl(f, std::make_integer_sequence<int, N>{}); }

constexpr int NT = 2, SC1 = 16;
constexpr int FF = 1376, FROW = 1440;
constexpr unsigned WS_EPOCH = 0;
constexpr unsigned WS_ML = 4096;
constexpr unsigned WS_TM = WS_ML + 64 * 128;
constexpr unsigned WS_QX = WS_TM + 2048;
constexpr unsigned WS_FX = WS_QX + 32 * 8 * 1024;
constexpr unsigned WS_Y = 1u << 20;
static_assert(WS_FX + 2 * 2 * 16 * FROW * 4 <= WS_Y, "ws map");
constexpr unsigned WS_END = WS_Y + 2 * 128 * 2048;
constexpr unsigned OOB = 0x7ff00000u;
constexpr int SPIN_CAP = 1 << 18;
constexpr int L_PL = 0, L_XN = 8192, L_Q = L_XN + 2048, L_H = L_Q + 256, L_ML = L_H + 32, L_MR = L_ML + 64, L_F = L_MR + 16,
              F_PL = L_F, F_Q = L_F + 4, F_ML = L_F + 8, F_MR = L_F + 12, F_KM = L_F + 16, F_XN = L_F + 20, L_T2 = L_F + 28,
              L_TAG = L_F + 32, L_TB = L_F + 44, L_RL = L_F + 80, L_OL = L_RL + 512, OL_STR = 144, L_END = L_OL + 16 * OL_STR;

enum { T_P, T_PF, T_K, T_V, T_WO, T_WD };
struct Item { int t, i; };
struct Act { int issue; int t, i; int k0, k1; };
constexpr int weight(int t) { return (t == T_WO || t == T_WD) ? 4 : 8; }
struct Sched {
  Act a[240]; int n;
  constexpr Sched() : a{}, n(0) {
    Item it[120] = {}; int ni = 0;
    for (int i = 0; i < 4; ++i) it[ni++] = Item{T_P, i};
    for (int i = 0; i < 3; ++i) it[ni++] = Item{T_PF, i};
    int wo = 0;
    for (int k = 0; k < 64; ++k) {
      it[ni++] = k < 32 ? Item{T_K, k} : Item{T_V, k - 32};
      if (k >= WD_POS && k < WD_POS + 4) it[ni++] = Item{T_WD, k - WD_POS};
      while (wo < 32 && k >= 3 && wo * 56 < (k - 2) * 32) it[ni++] = Item{T_WO, wo++};
    }
    int head = 0, fl = 0, kdone = 0, karr = 0;
    for (int j = 0; j <= ni; ++j) {
      while (head < j && (j == ni || fl + weight(it[j].t) > (head < 4 ? 48 : WLIM))) {
        Item r = it[head++]; fl -= weight(r.t);
        if (r.t == T_K) karr = r.i + 1;
        int k0 = kdone;
        if (karr > QPOS) kdone = (karr == 32 || kdone + 5 >= karr) ? karr : kdone + 5;
        a[n++] = Act{0, r.t, r.i, k0, kdone};
      }
      if (j < ni) { a[n++] = Act{1, it[j].t, it[j].i, 0, 0}; fl += weight(it[j].t); }
    }
  }
};

static_assert(WD_POS > QPOS, "h is written with q");

__device__ __forceinline__ float bflo(unsigned u) { return __uint_as_float(u << 16); }
__device__ __forceinline__ float bfhi(unsigned u) { return __uint_as_float(u & 0xffff0000u); }
__device__ __forceinline__ unsigned pkbf(float lo, float hi) { f32x2 f = {lo, hi}; bf16x2 h = __builtin_convertvector(f, bf16x2); return __builtin_bit_cast(unsigned, h); }
__device__ __forceinline__ float rbf(float f) { return bflo(pkbf(f, f)); }
__device__ __forceinline__ float dot2(unsigned a, unsigned b, float c) { return __builtin_amdgcn_fdot2_f32_bf16(__builtin_bit_cast(bf16x2, a), __builtin_bit_cast(bf16x2, b), c, false); }
template <int CTRL> __device__ __forceinline__ unsigned dppu(unsigned x) { return __builtin_amdgcn_update_dpp(0u, x, CTRL, 0xf, 0xf, true); }
template <int CTRL> __device__ __forceinline__ float dppf(float x) { return __uint_as_float(dppu<CTRL>(__float_as_uint(x))); }
constexpr int XOR1 = 0xB1, XOR2 = 0x4E, HMIR = 0x141, ROR8 = 0x128, ROR4 = 0x124, BCAST = 0x150;
__device__ __forceinline__ float row_sum(float x) { x += dppf<XOR1>(x); x += dppf<XOR2>(x); x += dppf<HMIR>(x); x += dppf<ROR8>(x); return x; }
__device__ __forceinline__ float row_max(float x) { x = fmaxf(x, dppf<XOR1>(x)); x = fmaxf(x, dppf<XOR2>(x)); x = fmaxf(x, dppf<HMIR>(x)); x = fmaxf(x, dppf<ROR8>(x)); return x; }
__device__ __forceinline__ float sw16(float a, float b) { auto s = __builtin_amdgcn_permlane16_swap(__float_as_uint(a), __float_as_uint(b), false, false); return __uint_as_float(s[0]) + __uint_as_float(s[1]); }
__device__ __forceinline__ float sw32(float a, float b) { auto s = __builtin_amdgcn_permlane32_swap(__float_as_uint(a), __float_as_uint(b), false, false); return __uint_as_float(s[0]) + __uint_as_float(s[1]); }
__device__ __forceinline__ float x16_max(float x) { auto s = __builtin_amdgcn_permlane16_swap(__float_as_uint(x), __float_as_uint(x), false, false); return fmaxf(__uint_as_float(s[0]), __uint_as_float(s[1])); }
__device__ __forceinline__ float x32_max(float x) { auto s = __builtin_amdgcn_permlane32_swap(__float_as_uint(x), __float_as_uint(x), false, false); return fmaxf(__uint_as_float(s[0]), __uint_as_float(s[1])); }
__device__ __forceinline__ float wave_sum(float x) { x = row_sum(x); x = sw16(x, x); return sw32(x, x); }
__device__ __forceinline__ float wave_max(float x) { return x32_max(x16_max(row_max(x))); }
__device__ __forceinline__ float tr4(f32x4 z) { return sw16(sw32(z.x, z.z), sw32(z.y, z.w)); }
__device__ __forceinline__ f32x4 mfma32(u32x4 a, u32x4 b, f32x4 c) { return __builtin_amdgcn_mfma_f32_16x16x32_bf16(__builtin_bit_cast(bf16x8, a), __builtin_bit_cast(bf16x8, b), c, 0, 0, 0); }
__device__ __forceinline__ f32x4 mfma16(u32x2 a, u32x2 b, f32x4 c) { return __builtin_amdgcn_mfma_f32_16x16x16bf16_1k(__builtin_bit_cast(s16x4, a), __builtin_bit_cast(s16x4, b), c, 0, 0, 0); }
__device__ __forceinline__ f32x4 smfma64(u32x4 a, u32x8 b, f32x4 c, int idx) { return __builtin_amdgcn_smfmac_f32_16x16x64_bf16(__builtin_bit_cast(bf16x8, a), __builtin_bit_cast(bf16x16, b), c, idx, 0, 0); }
__device__ __forceinline__ u32x4 ld16(rsrc_t r, unsigned v, unsigned s, int aux) { return aux == NT ? __builtin_amdgcn_raw_buffer_load_b128(r, v, s, NT) : aux == SC1 ? __builtin_amdgcn_raw_buffer_load_b128(r, v, s, SC1) : __builtin_amdgcn_raw_buffer_load_b128(r, v, s, 0); }
__device__ __forceinline__ void wg_barrier() { asm volatile("s_waitcnt lgkmcnt(0)\n\ts_barrier" ::: "memory"); }
__device__ __forceinline__ bool any(bool p) { return __builtin_amdgcn_ballot_w64(p) != 0; }
__device__ __forceinline__ unsigned now() { return (unsigned)__builtin_amdgcn_s_memrealtime(); }
__device__ __forceinline__ unsigned tagged(float v, unsigned m, unsigned t) { return ((__float_as_uint(v) + ((m + 1) >> 1)) & ~m) | (t & m); }
__device__ __forceinline__ u32x4 tagged4(f32x4 v, unsigned m, unsigned t) { return u32x4{tagged(v.x, m, t), tagged(v.y, m, t), tagged(v.z, m, t), tagged(v.w, m, t)}; }
__device__ __forceinline__ unsigned mism(u32x4 v, unsigned t) { return ((v.x ^ t) | (v.y ^ t)) | ((v.z ^ t) | (v.w ^ t)); }
__device__ __forceinline__ f32x4 untag(u32x4 v, unsigned m) { return __builtin_bit_cast(f32x4, v & ~m); }
#define CLOB() asm volatile("" ::: "memory")
#define TOUCH(x) asm volatile("" :: "v"(x) : "memory")
#define PIN(x) asm volatile("" : "+v"(x))
#define FENCE() __builtin_amdgcn_sched_barrier(0)
#define LDSW(off) (*(volatile __attribute__((address_space(3))) unsigned*)(lds + (off)))
constexpr int brev4(int t) { return ((t & 1) << 3) | ((t & 2) << 1) | ((t & 4) >> 1) | ((t & 8) >> 3); }
constexpr float LOG2E = 1.4426950408889634f;
__device__ __forceinline__ void lds_wait(unsigned char* lds, int off, unsigned want) {
  int spins = 0;
  while (LDSW(off) != want && spins < (SPIN_CAP << 4)) { ++spins; __builtin_amdgcn_s_sleep(1); }
  CLOB();
}
__device__ __forceinline__ void lds_add(unsigned char* lds, int off, unsigned v) { CLOB(); __hip_atomic_fetch_add((unsigned*)(lds + off), v, __ATOMIC_RELAXED, __HIP_MEMORY_SCOPE_WORKGROUP); CLOB(); }
template <int DUE> __device__ __forceinline__ void pace(unsigned vzero) {
  unsigned tv;
  asm volatile("ds_read_b32 %0, %1 offset:%c2\n"
               "P_%=:\n\ts_memrealtime vcc\n\ts_waitcnt lgkmcnt(0)\n\tv_readfirstlane_b32 vcc_hi, %0\n\ts_sub_i32 vcc_lo, vcc_lo, vcc_hi\n\t"
               "s_cmpk_lt_i32 vcc_lo, %c3\n\ts_cbranch_scc1 P_%="
               : "=&v"(tv) : "v"(vzero), "i"(L_TB), "i"(DUE) : "vcc", "scc", "memory");
}

enum { A_X, A_WQ, A_WG, A_WU, A_K, A_WS, A_V, A_WO, A_WD, A_OUT };
#define DESC(n) const void* p##n, unsigned n##n, unsigned f##n
__global__ void __launch_bounds__(576, 3)
fused_decode(DESC(x), DESC(wq), DESC(wg), DESC(wu), DESC(k), DESC(ws), DESC(v), DESC(wo), DESC(wd), DESC(out)) {
  __shared__ __attribute__((aligned(16))) unsigned char lds[L_END];
  const auto* ka = (const __attribute__((address_space(4))) rsrc_t*)__builtin_amdgcn_kernarg_segment_ptr();
  int tid = threadIdx.x, bid = blockIdx.x;
  rsrc_t rX, rP, rWO, rWS; u64 t0; int wv, h, par, span, ps1, ps2, ps3; unsigned poff, pfoff, ep, cpf; u32x4 x0, x1;
  { u64 pc; int t1, t2, t3, t4;
    asm volatile(
      "s_load_dwordx4 %[rX], %[ka], 0x0\n\t"
      "s_load_dwordx4 %[rWO], %[ka], 0x70\n\t"
      "s_load_dwordx4 %[rWS], %[ka], 0x50\n\t"
      "s_memrealtime %[t0]\n\t"
      "v_readfirstlane_b32 %[wv], %[tid]\n\t"
      "s_and_b32 %[t1], %[bid], 7\n\t"
      "s_lshr_b32 %[t2], %[bid], 6\n\t"
      "s_lshl2_add_u32 %[h], %[t1], %[t2]\n\t"
      "s_lshr_b32 %[wv], %[wv], 6\n\t"
      "s_cbranch_scc1 S_%=\n\t"
      "s_setprio 3\n\t"
      "s_getpc_b64 %[pc]\n\t"
      "s_lshr_b32 %[t1], %[bid], 3\n\t"
      "v_lshl_add_u32 %[poff], %[t1], 2, %[tid]\n\t"
      "v_min_u32 %[poff], %c[cl], %[poff]\n\t"
      "v_lshlrev_b32 %[poff], 7, %[poff]\n\t"
      "s_mov_b64 exec, 15\n\t"
      "global_load_dword %[cpf], %[poff], %[pc]\n\t"
      "s_mov_b64 exec, -1\n\t"
      "v_mov_b32 %[poff], 0\n\t"
      "v_lshlrev_b32 %[pfoff], 2, %[tid]\n\t"
      "ds_write_b32 %[pfoff], %[poff] offset:%c[lf]\n\t"
      "s_waitcnt lgkmcnt(0)\n\t"
      "v_lshlrev_b32 %[poff], 5, %[tid]\n\t"
      "s_buffer_load_dword %[ep], %[rWS], 0x0\n\t"
      "buffer_load_dwordx4 %[x0], %[poff], %[rX], 0 offen\n\t"
      "buffer_load_dwordx4 %[x1], %[poff], %[rX], 0 offen offset:16\n\t"
      "s_barrier\n\t"
      "s_branch E_%=\n"
      "S_%=:\n\t"
      "s_cmp_lt_u32 %[wv], 5\n\t"
      "s_cbranch_scc1 Q_%=\n\t"
      "s_cmpk_ge_u32 %[bid], 176\n\t"
      "s_cselect_b32 %[ps3], 0x30, 0x20\n\t"
      "s_cselect_b32 %[ps1], 11, 0\n\t"
      "s_lshr_b32 %[t4], %[bid], 4\n\t"
      "s_sub_u32 %[t4], %[t4], %[ps1]\n\t"
      "s_bfe_u32 %[t3], %[bid], 0x20003\n\t"
      "s_lshl2_add_u32 %[t2], %[t2], %[t3]\n\t"
      "s_add_u32 %[t2], %[t2], 5\n\t"
      "s_bfe_u32 %[ps1], %[bid], 0x10005\n\t"
      "s_lshl1_add_u32 %[t1], %[t1], %[ps1]\n\t"
      "s_cmp_ge_u32 %[wv], 7\n\t"
      "s_cselect_b32 %[ps3], 0x30, %[ps3]\n\t"
      "s_cselect_b32 %[t4], %[t2], %[t4]\n\t"
      "s_cselect_b32 %[t1], %[t1], %[bid]\n\t"
      "s_load_dwordx4 %[rP], %[ka], %[ps3]\n\t"
      "s_and_b32 %[par], %[t1], 1\n\t"
      "s_and_b32 %[span], %[t1], 14\n\t"
      "s_andn2_b32 %[t1], 1, %[wv]\n\t"
      "s_or_b32 %[span], %[span], %[t1]\n\t"
      "s_lshl_b32 %[t4], %[t4], 2\n\t"
      "s_sub_u32 %[t4], %[t4], %[par]\n\t"
      "s_lshl_b32 %[t4], %[t4], 6\n\t"
      "s_movk_i32 %[t2], 2752\n\t"
      "s_branch J_%=\n"
      "Q_%=:\n\t"
      "s_load_dwordx4 %[rP], %[ka], 0x10\n\t"
      "s_bfe_u32 %[par], %[bid], 0x10003\n\t"
      "s_lshr_b32 %[t1], %[bid], 2\n\t"
      "s_and_b32 %[t1], %[t1], 12\n\t"
      "s_add_u32 %[span], %[t1], %[wv]\n\t"
      "s_sub_u32 %[span], %[span], 1\n\t"
      "s_lshl_b32 %[t4], %[h], 8\n\t"
      "s_movk_i32 %[t2], 8192\n"
      "J_%=:\n\t"
      "v_lshrrev_b32 %[poff], 1, %[tid]\n\t"
      "s_lshl_b32 %[t1], %[span], 5\n\t"
      "v_and_b32 %[pfoff], 15, %[tid]\n\t"
      "s_or_b32 %[t1], %[t1], %[par]\n\t"
      "v_lshl_add_u32 %[pfoff], %[pfoff], 4, %[t4]\n\t"
      "v_and_or_b32 %[poff], %[poff], 24, %[t1]\n\t"
      "s_bfe_u32 %[t1], %[bid], 0x30003\n\t"
      "v_cmp_gt_u32 vcc, %[t2], %[pfoff]\n\t"
      "s_lshl3_add_u32 %[t1], %[h], %[t1]\n\t"
      "v_mad_u32_u24 %[poff], %[poff], %[t2], %[pfoff]\n\t"
      "v_mov_b32 %[pfoff], 0x7ff00000\n\t"
      "s_lshl_b32 %[t1], %[t1], 14\n\t"
      "v_cndmask_b32 %[poff], %[pfoff], %[poff], vcc\n\t"
      "s_sub_u32 %[t1], %[t1], 0x400\n\t"
      "s_lshl_b32 %[ps1], %[t2], 1\n\t"
      "v_lshl_add_u32 %[pfoff], %[tid], 4, %[t1]\n\t"
      "s_lshl_b32 %[ps2], %[t2], 2\n\t"
      "s_waitcnt lgkmcnt(0)\n\t"
      "s_mul_i32 %[ps3], %[t2], 6\n"
      "E_%=:"
      : [rX] "=&s"(rX), [rP] "=&s"(rP), [rWO] "=&s"(rWO), [rWS] "=&s"(rWS), [t0] "=&s"(t0), [wv] "=&s"(wv), [h] "=&s"(h), [par] "=&s"(par), [span] "=&s"(span),
        [ps1] "=&s"(ps1), [ps2] "=&s"(ps2), [ps3] "=&s"(ps3), [poff] "=&v"(poff), [pfoff] "=&v"(pfoff), [cpf] "=&v"(cpf), [x0] "=&v"(x0), [x1] "=&v"(x1), [ep] "=&s"(ep),
        [pc] "=&s"(pc), [t1] "=&s"(t1), [t2] "=&s"(t2), [t3] "=&s"(t3), [t4] "=&s"(t4)
      : [ka] "s"(ka), [bid] "s"(bid), [tid] "v"(tid), [cl] "i"(CODE_LINES - 1), [lf] "i"(L_F)
      : "vcc", "scc", "memory");
  }
  asm("" : "+s"(rX)); asm("" : "+s"(rWO)); asm("" : "+s"(rP)); asm("" : "+s"(rWS)); asm("" : "+s"(wv)); asm("" : "+s"(h)); asm("" : "+s"(par)); asm("" : "+s"(span)); asm("" : "+s"(t0));
  static_assert(WS_EPOCH == 0 && 2 * FF == 2752 && OOB == 0x7ff00000u && A_WO == 7 && A_WS == 5 && L_F + 256 <= L_END, "constants of the block above");
#define INDICES   \
  asm volatile("" : "+s"(bid), "+s"(wv)); asm volatile("" : "+v"(tid)); \
  const int lane = tid & 63, g = lane >> 4, c = lane & 15, xcd = bid & 7, j = bid >> 3; \
  const int hq = j >> 3, b = (j >> 2) & 1, s = j & 3, i16 = hq * 4 + s; \
  const int dcnt = i16 < 3 ? 4 : i16 < 6 ? 8 : i16 < 12 ? 12 : 16, dstart = xcd * 172 + dcnt * i16 - (i16 < 3 ? 0 : i16 < 6 ? 12 : i16 < 12 ? 36 : 84)

  if (wv == 0) {
    INDICES;
    const unsigned tstart = (unsigned)t0;
    if (lane < 9) LDSW(L_TB + lane * 4) = tstart - (1u << 30);
    FENCE();
    {
      float e[16];
      asm volatile("s_waitcnt vmcnt(0)" : "+v"(x0), "+v"(x1), "+v"(cpf) :: "memory");
      sfor<4>([&](auto ec) INL { constexpr int k = decltype(ec)::v; const unsigned u0 = x0[k], u1 = x1[k];
        e[2 * k] = bflo(u0); e[2 * k + 1] = bfhi(u0); e[8 + 2 * k] = bflo(u1); e[9 + 2 * k] = bfhi(u1); });
      float a = (((e[0] + e[1]) + (e[2] + e[3])) + ((e[4] + e[5]) + (e[6] + e[7]))) + (((e[8] + e[9]) + (e[10] + e[11])) + ((e[12] + e[13]) + (e[14] + e[15])));
      a = row_sum(a); a = sw16(a, a) * (1.f / 512.f);
      float v0 = 0.f, v1 = 0.f;
      sfor<8>([&](auto ec) INL { constexpr int k = decltype(ec)::v; const float d0 = e[k] - a, d1 = e[8 + k] - a; v0 = fmaf(d0, d0, v0); v1 = fmaf(d1, d1, v1); });
      float v = row_sum(v0 + v1); v = sw16(v, v) * (1.f / 512.f);
      const float mean = rbf(a), rden = __builtin_amdgcn_rcpf(rbf(__builtin_amdgcn_sqrtf(rbf(rbf(v) + 1e-5f))));
      u32x4 n0, n1;
      sfor<4>([&](auto ec) INL { constexpr int k = decltype(ec)::v;
        const unsigned d0 = pkbf(e[2 * k] - mean, e[2 * k + 1] - mean), d1 = pkbf(e[8 + 2 * k] - mean, e[9 + 2 * k] - mean);
        n0[k] = pkbf(bflo(d0) * rden, bfhi(d0) * rden); n1[k] = pkbf(bflo(d1) * rden, bfhi(d1) * rden); });
      *(u32x4*)(lds + L_XN + lane * 32) = n0; *(u32x4*)(lds + L_XN + lane * 32 + 16) = n1;
    }
    CLOB(); LDSW(F_XN) = 1u;
    FENCE();
    asm volatile("s_waitcnt lgkmcnt(0)" : "+s"(ep), "+s"(par) :: "memory");
    const rsrc_t rV = ka[A_V], rOut = ka[A_OUT];
    unsigned ooff = (lane < 16 && s == 0) ? lane * 2 : OOB;
    const unsigned xr = __builtin_amdgcn_raw_buffer_load_b32(rX, ooff & ~2u, b * 1024 + h * 32, 0);
    const unsigned tl0 = __builtin_amdgcn_raw_buffer_load_b32(rV, 0, ((b * 32 + h) * 4 + s) << 18, NT), tl1 = __builtin_amdgcn_raw_buffer_load_b32(rOut, ooff & ~2u, b * 1024 + h * 32, 0);
    const unsigned tag = ((ep * 747796405u + 2891336453u) & ~0xfu) | ((ep + 1u) & 0xfu);
    LDSW(L_TAG) = tag;
    const int mA = bid >= 176, cA = (bid >> 4) - 11 * mA, sA = bid & 15, cB = 5 + i16, sB = 2 * xcd + b;
    const unsigned dQ = WS_QX + (h * 8 + (j & 7)) * 1024, dA = WS_FX + (mA * 16 + sA) * (2 * FROW * 4) + (cA * 128 + 32 - 32 * (sA & 1)) * 4,
                   dB = WS_FX + (16 + sB) * (2 * FROW * 4) + (cB * 128 + 32 - 32 * (sB & 1)) * 4;
    unsigned fvo = (lane & 31) * 16 + (lane >> 5) * (FROW * 4), qo = (lane >> 5) * 1024 + (lane & 31) * 16;
    const bool fon = 4 * (lane & 3) < dcnt;
    unsigned fo = fon ? (lane >> 2) * (2 * FROW * 4) + (lane & 3) * 16 : OOB, fmask = fon ? 0xffu : 0u;
    const unsigned fbase = WS_FX + b * (FROW * 4) + (32 + dstart) * 4;
    PIN(fvo); PIN(qo); PIN(fo); PIN(fmask);
    lds_wait(lds, F_PL, 8);
    { const unsigned char* src = lds + L_PL + lane * 16;
      const f32x4 vq = (*(const f32x4*)src + *(const f32x4*)(src + 1024)) + (*(const f32x4*)(src + 2048) + *(const f32x4*)(src + 3072));
      const f32x4 vA = *(const f32x4*)(src + 4096) + *(const f32x4*)(src + 5120), vB = *(const f32x4*)(src + 6144) + *(const f32x4*)(src + 7168);
      __builtin_amdgcn_raw_buffer_store_b128(tagged4(vq, 3u, tag), rWS, lane * 16, dQ, SC1);
      __builtin_amdgcn_raw_buffer_store_b128(tagged4(vA, 0xffu, tag), rWS, fvo, dA, SC1);
      __builtin_amdgcn_raw_buffer_store_b128(tagged4(vB, 0xffu, tag), rWS, i16 < 6 ? fvo : OOB, dB, SC1);
    }
    asm volatile("s_waitcnt vmcnt(0)" ::: "memory");
    { u32x4 qp[4], fg, fu; int spins = 0;
      _Pragma("unroll 1") do {
        CLOB();
        sfor<4>([&](auto kc) INL { constexpr int k = decltype(kc)::v; qp[k] = ld16(rWS, qo, WS_QX + h * 8192 + b * 512 + k * 2048, SC1); });
        fg = ld16(rWS, fo, fbase, SC1); fu = ld16(rWS, fo, fbase + 16 * 2 * FROW * 4, SC1);
      } while (any((((mism(qp[0], tag) | mism(qp[1], tag) | mism(qp[2], tag) | mism(qp[3], tag)) & 3u) | ((mism(fg, tag) | mism(fu, tag)) & fmask)) != 0) && ++spins < SPIN_CAP);
      const f32x4 qs = (untag(qp[0], 3u) + untag(qp[1], 3u)) + (untag(qp[2], 3u) + untag(qp[3], 3u));
      *(u32x2*)(lds + L_Q + (lane & 31) * 8) = u32x2{pkbf(sw32(qs.x, qs.x), sw32(qs.y, qs.y)), pkbf(sw32(qs.z, qs.z), sw32(qs.w, qs.w))};
      float gv = tr4(untag(fg, 0xffu)), uv = tr4(untag(fu, 0xffu));
      gv += dppf<ROR8>(gv); uv += dppf<ROR8>(uv); gv += dppf<ROR4>(gv); uv += dppf<ROR4>(uv);
      gv = rbf(gv); uv = rbf(uv);
      const float z = 0.7978845608028654f * (gv + 0.044715f * gv * gv * gv);
      const float ge = rbf(gv * __builtin_amdgcn_rcpf(1.f + __builtin_amdgcn_exp2f(-2.f * LOG2E * z)));
      if (c < 4) *(u16*)(lds + L_H + (4 * c + g) * 2) = (u16)pkbf(ge * uv, 0.f);
      CLOB(); LDSW(F_Q) = 1u;
    }
    TOUCH(tl0); TOUCH(tl1); TOUCH(xr);
    { lds_wait(lds, F_KM, 8);
      const unsigned tK = tstart + ((LDSW(L_T2) - 8u * tstart) >> 3);
      __builtin_amdgcn_raw_buffer_store_b64(u32x2{tK, tag}, rWS, (lane == 0 && s == hq && b == (hq & 1)) ? 0u : OOB, WS_TM + h * 8, SC1);
      for (int k = 0; k < TDELAY; ++k) __builtin_amdgcn_s_sleep(16);
      const int dn = (int)(now() - tK);
      const u32x2 ta = __builtin_amdgcn_raw_buffer_load_b64(rWS, (lane & 31) * 8, WS_TM, SC1);
      const float mt = wave_sum((float)(ta.y == tag ? (int)(ta.x - tK) : dn)) * (1.f / 64.f);
      if (lane < 9) LDSW(L_TB + lane * 4) = tK + (int)fminf(mt, 400.f) + (lane ? ((2 * lane - 9) * STAGW) >> 5 : 0);
    }
    { const unsigned mb = WS_ML + (b * 32 + h) * 128;
      lds_wait(lds, F_ML, 8);
      const f32x2 ml = *(const f32x2*)(lds + L_ML + (lane & 7) * 8);
      float m = fmaxf(ml.x, dppf<XOR1>(ml.x)); m = fmaxf(m, dppf<XOR2>(m)); m = fmaxf(m, dppf<HMIR>(m));
      float l = ml.y * __builtin_amdgcn_exp2f((ml.x - m) * LOG2E); l += dppf<XOR1>(l); l += dppf<XOR2>(l); l += dppf<HMIR>(l);
      __builtin_amdgcn_raw_buffer_store_b128(u32x4{__float_as_uint(m), tag, __float_as_uint(l), tag}, rWS, lane == 0 ? 0u : OOB, mb + s * 16, SC1);
      __builtin_amdgcn_s_sleep(127);
      u32x4 mp; int spins = 0;
      _Pragma("unroll 1") do {
        __builtin_amdgcn_s_sleep(8); CLOB();
        mp = ld16(rWS, (lane & 3) * 16, mb, SC1);
      } while (any(mp.y != tag || mp.w != tag) && ++spins < SPIN_CAP);
      const float mj = __uint_as_float(mp.x), lj = __uint_as_float(mp.z);
      float M = fmaxf(mj, dppf<XOR1>(mj)); M = fmaxf(M, dppf<XOR2>(M));
      float L = lj * __builtin_amdgcn_exp2f((mj - M) * LOG2E); L += dppf<XOR1>(L); L += dppf<XOR2>(L);
      *(f32x2*)(lds + L_MR) = f32x2{M, __builtin_amdgcn_rcpf(L)};
      CLOB(); LDSW(F_MR) = 1u;
    }
    unsigned rrd = L_RL + c * 32; float xf = (lane & 1) ? bfhi(xr) : bflo(xr);
    PIN(rrd); PIN(xf); PIN(ooff);
    wg_barrier();
    if (s != 0) return;
    wg_barrier();
    const f32x4 r4 = *(const f32x4*)(lds + rrd) + *(const f32x4*)(lds + rrd + 16);
    __builtin_amdgcn_raw_buffer_store_b16((u16)pkbf(xf + ((r4.x + r4.y) + (r4.z + r4.w)), 0.f), rOut, ooff, b * 1024 + h * 32, 0);
    if (bid == 0) __builtin_amdgcn_raw_buffer_store_b32(tag, rWS, lane == 0 ? 0u : OOB, WS_EPOCH, 0);
    return;
  }

  u32x4 P[4], PF[3], KV[64];
  P[0] = ld16(rP, poff, 0, NT); P[1] = ld16(rP, poff, ps1, NT); P[2] = ld16(rP, poff, ps2, NT); P[3] = ld16(rP, poff, ps3, NT);
  asm volatile("s_barrier" ::: "memory");
  PF[0] = ld16(rWO, pfoff, 0, 0); PF[1] = ld16(rWO, pfoff, 8192, 0);
  CLOB(); FENCE();
  const rsrc_t rK = ka[A_K], rV = ka[A_V], rWD = ka[A_WD];
  INDICES;
  const int w = wv - 1, rot = (bid << 12) & 0x38000;
  unsigned kvoff = (((b * 32 + h) * 4 + s) << 18) + (tid - 64) * 16; PIN(kvoff);
  u32x4 sel, q;
  u32x2 WO[32], WD[4];
  u32x8 BO[8];
  unsigned wooff = 0, wdoff = 0, owr = 0, ord = 0, yst = 0, yld = 0, rwr = 0, ytag = 0, vzero = 0, vw4 = wv * 4;
  int sidx = 0;
  f32x4 oacc = {0.f, 0.f, 0.f, 0.f}, yacc[4];
  float t1 = 0.f, t2 = 0.f, t3 = 0.f, t4 = 0.f, sc[2] = {0.f, 0.f}, mw = 0.f, cw = 0.f;
  unsigned pp[2] = {0u, 0u};
  auto mkA = [&](unsigned src) INL -> u32x4 { u32x4 a; a.x = __builtin_amdgcn_perm(src, src, sel.x); a.y = __builtin_amdgcn_perm(src, src, sel.y);
                                              a.z = __builtin_amdgcn_perm(src, src, sel.z); a.w = __builtin_amdgcn_perm(src, src, sel.w); return a; };
  auto tile_off = [&](int t) INL { int r = rot; asm volatile("" : "+s"(r)); return (r + t * 8192) & 0x3ffff; };

  auto consumeK = [&](auto tc) INL {
    constexpr int t = decltype(tc)::v;
    const u32x4 k = KV[t];
    float d = dot2(k.x, q.x, 0.f); d = dot2(k.y, q.y, d); d = dot2(k.z, q.z, d); d = dot2(k.w, q.w, d);
    if constexpr ((t & 1) == 0) { t1 = d; PIN(t1); }
    else {
      { const float keep = (c & 8) ? d : t1, send = (c & 8) ? t1 : d; d = keep + dppf<ROR8>(send); }
      if constexpr ((t & 2) == 0) { t2 = d; PIN(t2); }
      else {
        { const float keep = (c & 4) ? d : t2, send = (c & 4) ? t2 : d; d = keep + dppf<HMIR>(send); }
        if constexpr ((t & 4) == 0) { t3 = d; PIN(t3); }
        else {
          { const float keep = (c & 2) ? d : t3, send = (c & 2) ? t3 : d; d = keep + dppf<XOR2>(send); }
          if constexpr ((t & 8) == 0) { t4 = d; PIN(t4); }
          else { const float keep = (c & 1) ? d : t4, send = (c & 1) ? t4 : d; sc[t >> 4] = keep + dppf<XOR1>(send); PIN(sc[t >> 4]); }
        }
      }
    }
    if constexpr (t == 31) {
      const float s0 = rbf(rbf(sc[0]) * 0.08838834764831845f), s1 = rbf(rbf(sc[1]) * 0.08838834764831845f);
      mw = wave_max(fmaxf(s0, s1));
      const float p0 = __builtin_amdgcn_exp2f((s0 - mw) * LOG2E), p1 = __builtin_amdgcn_exp2f((s1 - mw) * LOG2E);
      const float lw = wave_sum(p0 + p1);
      pp[0] = pkbf(p0, p0 - rbf(p0)); pp[1] = pkbf(p1, p1 - rbf(p1)); PIN(pp[0]); PIN(pp[1]);
      if (lane == 0) { *(f32x2*)(lds + L_ML + w * 8) = f32x2{mw, lw}; lds_add(lds, F_ML, 1u); }
    }
  };

  constexpr Sched S{};
  sfor<S.n>([&](auto nc) INL {
    constexpr Act A = S.a[decltype(nc)::v];
    constexpr int i = A.i;
    if constexpr (A.issue) {
      if constexpr (A.t == T_K || A.t == T_V) {
        constexpr int n = i + (A.t == T_V ? 32 : 0);
        if constexpr (n >= APACE0) { pace<(AEND - (63 - n) * ATAU) / 16>(vw4); FENCE(); }
        KV[n] = ld16(A.t == T_V ? rV : rK, kvoff, tile_off(i), NT);
      }
      else if constexpr (A.t == T_PF && i == 2) PF[2] = ld16(rWD, 2 * w < dcnt ? (tid - 64) * 16 : OOB, (dstart + b * (dcnt >> 1)) * 1024, 0);
      else if constexpr (A.t == T_WO) WO[i] = __builtin_amdgcn_raw_buffer_load_b64(rWO, wooff, ((i >> 3) * 32 + (i & 7)) * 1024, NT);
      else if constexpr (A.t == T_WD) WD[i] = __builtin_amdgcn_raw_buffer_load_b64(rWD, wdoff, i * 1024, NT);
    } else {
      if constexpr (A.t == T_P) {
        if constexpr (i == 0) {
          const unsigned sb = (c & 8) ? 0x0302u : 0x0100u, one = (c & 1) ? ((sb << 16) | 0x0c0cu) : (0x0c0c0000u | sb); const int v = (c & 7) >> 1;
          sel.x = v == 0 ? one : 0x0c0c0c0cu; sel.y = v == 1 ? one : 0x0c0c0c0cu; sel.z = v == 2 ? one : 0x0c0c0c0cu; sel.w = v == 3 ? one : 0x0c0c0c0cu;
          wooff = h * 131072 + g * 8192 + w * 128 + c * 8;
          wdoff = (4 * g < dcnt) ? (unsigned)(dstart * 1024 + g * 4096 + w * 128 + c * 8) : OOB;
          owr = L_OL + (w + (lane >= 32 ? 8 : 0)) * OL_STR + c * 8 + (g & 1) * 4;
          ord = L_OL + c * OL_STR + g * 16;
          sidx = (c & 8) ? 0xeeee : 0x4444;
          yst = g ? OOB : WS_Y + (b * 128 + h * 4 + s) * 2048 + w * 256 + c * 16;
          yld = WS_Y + b * 128 * 2048 + h * 64 + ((tid - 64) >> 2) * 2048 + (lane & 3) * 16;
          rwr = c < 4 ? L_RL + (4 * c + g) * 32 + w * 4 : L_PL + lane * 4;
          PIN(sel); PIN(wooff); PIN(wdoff); PIN(owr); PIN(ord); PIN(sidx); PIN(yst); PIN(yld); PIN(rwr); PIN(vzero); PIN(vw4);
        }
        TOUCH(P[i]);
        if constexpr (i == 3) {
          lds_wait(lds, F_XN, 1u);
          const u32x4 xa = *(const u32x4*)(lds + L_XN + (c >> 3) * 1024 + g * 16 + span * 64);
          f32x4 pacc = {0.f, 0.f, 0.f, 0.f};
          sfor<4>([&](auto ec) INL { constexpr int e = decltype(ec)::v; const unsigned u = xa[e];
            pacc = mfma32(mkA(__builtin_amdgcn_perm(u, u, par ? 0x03020302u : 0x01000100u)), P[e], pacc); });
          *(f32x4*)(lds + L_PL + w * 1024 + (g >> 1) * 512 + c * 32 + (g & 1) * 16) = pacc;
          if (lane == 0) lds_add(lds, F_PL, 1u);
        }
      } else if constexpr (A.t == T_PF) { TOUCH(PF[i]);
      } else if constexpr (A.t == T_K) {
        TOUCH(KV[i]);
        if constexpr (i == KM) { const unsigned t = now(); if (lane == 0) { lds_add(lds, L_T2, t); lds_add(lds, F_KM, 1u); } }
        if constexpr (i == QPOS) {
          lds_wait(lds, F_Q, 1u);
          q = *(const u32x4*)(lds + L_Q + c * 16);
          ytag = LDSW(L_TAG) & 0xfu;
          PIN(ytag);
        }
      } else if constexpr (A.t == T_WD) {
        TOUCH(WD[i]);
        if constexpr (i == 3) {
          u32x2 hA = *(const u32x2*)(lds + L_H + g * 8);
          if (c != 0) hA = u32x2{0u, 0u};
          sfor<4>([&](auto tc) INL { constexpr int t = decltype(tc)::v; constexpr unsigned ps = (t & 1) ? 0x07060302u : 0x05040100u;
            const unsigned w0 = WD[0][t >> 1], w1 = WD[1][t >> 1], w2 = WD[2][t >> 1], w3 = WD[3][t >> 1];
            const u32x2 bb = {__builtin_amdgcn_perm(w1, w0, ps), __builtin_amdgcn_perm(w3, w2, ps)};
            yacc[t] = mfma16(hA, bb, f32x4{0.f, 0.f, 0.f, 0.f}); PIN(yacc[t]); });
        }
      } else if constexpr (A.t == T_WO) {
        TOUCH(WO[i]);
        if constexpr ((i & 7) == 7) {
          constexpr int u = i >> 3;
          sfor<4>([&](auto tc) INL { constexpr int t = decltype(tc)::v; constexpr unsigned ps = (t & 1) ? 0x07060302u : 0x05040100u;
            sfor<4>([&](auto vc) INL { constexpr int v = decltype(vc)::v;
              const unsigned lo = WO[u * 8 + 2 * v][t >> 1], hi = WO[u * 8 + 2 * v + 1][t >> 1];
              BO[(u >> 1) * 4 + t][(u & 1) * 4 + v] = __builtin_amdgcn_perm(hi, lo, ps); });
            PIN(BO[(u >> 1) * 4 + t]); });
        }
      } else if constexpr (A.t == T_V) {
        oacc = mfma32(mkA(dppu<BCAST + brev4(i & 15)>(pp[i >> 4])), KV[32 + i], oacc); PIN(oacc);
        if constexpr (i == 27) {
          lds_wait(lds, F_MR, 1u);
          const f32x2 mr = *(const f32x2*)(lds + L_MR);
          cw = __builtin_amdgcn_exp2f((mw - mr.x) * LOG2E) * mr.y;
          PIN(cw);
        }
      }
      sfor<A.k1 - A.k0>([&](auto kc) INL { FENCE(); consumeK(IC<A.k0 + decltype(kc)::v>{}); });
    }
    FENCE();
  });

  *(unsigned*)(lds + owr) = pkbf(sw32(oacc.x, oacc.z) * cw, sw32(oacc.y, oacc.w) * cw);
  wg_barrier();
  { const u32x4 a0 = *(const u32x4*)(lds + ord), a1 = *(const u32x4*)(lds + ord + 64);
    FENCE();
    auto sum4 = [&](f32x4 u) INL { const f32x2 p = f32x2{u.x, u.y} + f32x2{u.z, u.w}; return p.x + p.y; };
    auto fin = [&](float y) INL { y = sw16(y, y); const unsigned t = ((__float_as_uint(y) + 8u) & ~0xfu) | ytag; return __builtin_amdgcn_permlane32_swap(t, t, false, false); };
    yacc[0] = smfma64(a0, BO[0], yacc[0], sidx); yacc[2] = smfma64(a0, BO[2], yacc[2], sidx); FENCE();
    yacc[0] = smfma64(a1, BO[4], yacc[0], sidx); yacc[2] = smfma64(a1, BO[6], yacc[2], sidx); FENCE();
    yacc[1] = smfma64(a0, BO[1], yacc[1], sidx); FENCE();
    float z0 = sum4(yacc[0]); PIN(z0); FENCE();
    yacc[3] = smfma64(a0, BO[3], yacc[3], sidx); FENCE();
    float z2 = sum4(yacc[2]); PIN(z2); FENCE();
    yacc[1] = smfma64(a1, BO[5], yacc[1], sidx); FENCE();
    float ya = sw32(z0, z2); PIN(ya); FENCE();
    yacc[3] = smfma64(a1, BO[7], yacc[3], sidx); FENCE();
    const auto sa = fin(ya);
    const auto sb = fin(sw32(sum4(yacc[1]), sum4(yacc[3])));
    __builtin_amdgcn_raw_buffer_store_b128(u32x4{sa[0], sb[0], sa[1], sb[1]}, rWS, yst, 0, SC1);
  }
  if (s != 0) return;
  { u32x4 y; int spins = SPIN_CAP;
    pace<AEND / 16 + YPOLL>(vzero);
    unsigned t0_, t1_;
    asm volatile("s_waitcnt vmcnt(0)\n"
                 "L_%=:\n\tbuffer_load_dwordx4 v[2:5], %[off], %[rs], 0 offen sc1\n\ts_waitcnt vmcnt(0)\n\t"
                 "v_bitop3_b32 %[t0], v2, v3, %[tag] bitop3:0x7e\n\tv_bitop3_b32 %[t1], v4, v5, %[tag] bitop3:0x7e\n\t"
                 "v_bitop3_b32 %[t0], %[t0], %[t1], 15 bitop3:0xa8\n\tv_cmp_ne_u32 vcc, 0, %[t0]\n\ts_cbranch_vccz D_%=\n\t"
                 "s_sub_u32 %[sp], %[sp], 1\n\ts_cbranch_scc0 L_%=\n"
                 "D_%=:"
                 : "=&{v[2:5]}"(y), [t0] "=&v"(t0_), [t1] "=&v"(t1_), [sp] "+s"(spins) : [off] "v"(yld), [rs] "s"(rWS), [tag] "v"(ytag) : "vcc", "scc", "memory");
    float v = tr4(untag(y, 0xfu)); v += dppf<ROR8>(v); v += dppf<ROR4>(v);
    *(float*)(lds + rwr) = v;
    wg_barrier();
  }
}
}

extern "C" void kernel_launch(void* const* d_in, const int* in_sizes, int n_in, void* d_out, int out_size, void* d_ws, size_t ws_size, hipStream_t stream) {
  const unsigned F = 0x00020000u;
  fused_decode<<<256, 576, 0, stream>>>(d_in[0], 2048u, F, d_in[3], 512u * 8192u, F, d_in[6], 512u * 2u * FF, F, d_in[7], 512u * 2u * FF, F, d_in[1], 64u << 20, F,
                                        d_ws, WS_END, F, d_in[2], 64u << 20, F, d_in[4], 4096u * 1024u, F, d_in[8], FF * 1024u, F, d_out, 2048u, F);
}
```

```cpp
#define PROBE 99
#define PSEL (wv == 1)
#define PX -1
#define PSTR 0
#include <hip/hip_runtime.h>
#include <stdint.h>
#include <utility>

#define CODE_LINES 115
#define WLIM 40
#define QPOS 14
#define WD_POS 22
#define KM 16
#define TDELAY 5
#define APACE0 29
#define ATAU 450
#define AEND 19650
#define STAGW 56
#define SK 0
#define YPOLL 125

namespace {
typedef unsigned u32x8 __attribute__((ext_vector_type(8)));
typedef unsigned u32x4 __attribute__((ext_vector_type(4)));
typedef unsigned u32x2 __attribute__((ext_vector_type(2)));
typedef float f32x4 __attribute__((ext_vector_type(4)));
typedef float f32x2 __attribute__((ext_vector_type(2)));
typedef __bf16 bf16x16 __attribute__((ext_vector_type(16)));
typedef __bf16 bf16x8 __attribute__((ext_vector_type(8)));
typedef __bf16 bf16x2 __attribute__((ext_vector_type(2)));
typedef short s16x4 __attribute__((ext_vector_type(4)));
typedef unsigned short u16;
typedef unsigned long long u64;
typedef __amdgpu_buffer_rsrc_t rsrc_t;

#define INL __attribute__((always_inline))
template <int I> struct IC { static constexpr int v = I; };
template <class F, int... Is> __device__ __forceinline__ void sfor_impl(F&& f, std::integer_sequence<int, Is...>) { (f(IC<Is>{}), ...); }
template <int N, class F> __device__ __forceinline__ void sfor(F&& f) { sfor_impl(f, std::make_integer_sequence<int, N>{}); }

constexpr int NT = 2, SC1 = 16;
constexpr int FF = 1376, FROW = 1440;
constexpr unsigned WS_EPOCH = 0;
constexpr unsigned WS_ML = 4096;
constexpr unsigned WS_TM = WS_ML + 64 * 128;
constexpr unsigned WS_QX = WS_TM + 2048;
constexpr unsigned WS_FX = WS_QX + 32 * 8 * 1024;
constexpr unsigned WS_Y = 1u << 20;
static_assert(WS_FX + 2 * 2 * 16 * FROW * 4 <= WS_Y, "ws map");
constexpr unsigned WS_END = WS_Y + 2 * 128 * 2048;
constexpr unsigned OOB = 0x7ff00000u;
constexpr int SPIN_CAP = 1 << 18;
constexpr int L_PL = 0, L_XN = 8192, L_Q = L_XN + 2048, L_H = L_Q + 256, L_ML = L_H + 32, L_MR = L_ML + 64, L_F = L_MR + 16,
              F_PL = L_F, F_Q = L_F + 4, F_ML = L_F + 8, F_MR = L_F + 12, F_KM = L_F + 16, F_XN = L_F + 20, L_T2 = L_F + 28,
              L_TAG = L_F + 32, L_TB = L_F + 44, L_RL = L_F + 80, L_OL = L_RL + 512, OL_STR = 144, L_END = L_OL + 16 * OL_STR;

enum { T_P, T_PF, T_K, T_V, T_WO, T_WD };
struct Item { int t, i; };
struct Act { int issue; int t, i; int k0, k1; };
constexpr int weight(int t) { return (t == T_WO || t == T_WD) ? 4 : 8; }
struct Sched {
  Act a[240]; int n;
  constexpr Sched() : a{}, n(0) {
    Item it[120] = {}; int ni = 0;
    for (int i = 0; i < 4; ++i) it[ni++] = Item{T_P, i};
    for (int i = 0; i < 3; ++i) it[ni++] = Item{T_PF, i};
    int wo = 0;
    for (int k = 0; k < 64; ++k) {
      it[ni++] = k < 32 ? Item{T_K, k} : Item{T_V, k - 32};
      if (k >= WD_POS && k < WD_POS + 4) it[ni++] = Item{T_WD, k - WD_POS};
      while (wo < 32 && k >= 3 && wo * 56 < (k - 2) * 32) it[ni++] = Item{T_WO, wo++};
    }
    int head = 0, fl = 0, kdone = 0, karr = 0;
    for (int j = 0; j <= ni; ++j) {
      while (head < j && (j == ni || fl + weight(it[j].t) > (head < 4 ? 48 : WLIM))) {
        Item r = it[head++]; fl -= weight(r.t);
        if (r.t == T_K) karr = r.i + 1;
        int k0 = kdone;
        if (karr > QPOS) kdone = (karr == 32 || kdone + 5 >= karr) ? karr : kdone + 5;
        a[n++] = Act{0, r.t, r.i, k0, kdone};
      }
      if (j < ni) { a[n++] = Act{1, it[j].t, it[j].i, 0, 0}; fl += weight(it[j].t); }
    }
  }
};

static_assert(WD_POS > QPOS, "h is written with q");

__device__ __forceinline__ float bflo(unsigned u) { return __uint_as_float(u << 16); }
__device__ __forceinline__ float bfhi(unsigned u) { return __uint_as_float(u & 0xffff0000u); }
__device__ __forceinline__ unsigned pkbf(float lo, float hi) { f32x2 f = {lo, hi}; bf16x2 h = __builtin_convertvector(f, bf16x2); return __builtin_bit_cast(unsigned, h); }
__device__ __forceinline__ float rbf(float f) { return bflo(pkbf(f, f)); }
__device__ __forceinline__ float dot2(unsigned a, unsigned b, float c) { return __builtin_amdgcn_fdot2_f32_bf16(__builtin_bit_cast(bf16x2, a), __builtin_bit_cast(bf16x2, b), c, false); }
template <int CTRL> __device__ __forceinline__ unsigned dppu(unsigned x) { return __builtin_amdgcn_update_dpp(0u, x, CTRL, 0xf, 0xf, true); }
template <int CTRL> __device__ __forceinline__ float dppf(float x) { return __uint_as_float(dppu<CTRL>(__float_as_uint(x))); }
constexpr int XOR1 = 0xB1, XOR2 = 0x4E, HMIR = 0x141, ROR8 = 0x128, ROR4 = 0x124, BCAST = 0x150;
__device__ __forceinline__ float row_sum(float x) { x += dppf<XOR1>(x); x += dppf<XOR2>(x); x += dppf<HMIR>(x); x += dppf<ROR8>(x); return x; }
__device__ __forceinline__ float row_max(float x) { x = fmaxf(x, dppf<XOR1>(x)); x = fmaxf(x, dppf<XOR2>(x)); x = fmaxf(x, dppf<HMIR>(x)); x = fmaxf(x, dppf<ROR8>(x)); return x; }
__device__ __forceinline__ float sw16(float a, float b) { auto s = __builtin_amdgcn_permlane16_swap(__float_as_uint(a), __float_as_uint(b), false, false); return __uint_as_float(s[0]) + __uint_as_float(s[1]); }
__device__ __forceinline__ float sw32(float a, float b) { auto s = __builtin_amdgcn_permlane32_swap(__float_as_uint(a), __float_as_uint(b), false, false); return __uint_as_float(s[0]) + __uint_as_float(s[1]); }
__device__ __forceinline__ float x16_max(float x) { auto s = __builtin_amdgcn_permlane16_swap(__float_as_uint(x), __float_as_uint(x), false, false); return fmaxf(__uint_as_float(s[0]), __uint_as_float(s[1])); }
__device__ __forceinline__ float x32_max(float x) { auto s = __builtin_amdgcn_permlane32_swap(__float_as_uint(x), __float_as_uint(x), false, false); return fmaxf(__uint_as_float(s[0]), __uint_as_float(s[1])); }
__device__ __forceinline__ float wave_sum(float x) { x = row_sum(x); x = sw16(x, x); return sw32(x, x); }
__device__ __forceinline__ float wave_max(float x) { return x32_max(x16_max(row_max(x))); }
__device__ __forceinline__ float tr4(f32x4 z) { return sw16(sw32(z.x, z.z), sw32(z.y, z.w)); }
__device__ __forceinline__ f32x4 mfma32(u32x4 a, u32x4 b, f32x4 c) { return __builtin_amdgcn_mfma_f32_16x16x32_bf16(__builtin_bit_cast(bf16x8, a), __builtin_bit_cast(bf16x8, b), c, 0, 0, 0); }
__device__ __forceinline__ f32x4 mfma16(u32x2 a, u32x2 b, f32x4 c) { return __builtin_amdgcn_mfma_f32_16x16x16bf16_1k(__builtin_bit_cast(s16x4, a), __builtin_bit_cast(s16x4, b), c, 0, 0, 0); }
__device__ __forceinline__ f32x4 smfma64(u32x4 a, u32x8 b, f32x4 c, int idx) { return __builtin_amdgcn_smfmac_f32_16x16x64_bf16(__builtin_bit_cast(bf16x8, a), __builtin_bit_cast(bf16x16, b), c, idx, 0, 0); }
__device__ __forceinline__ u32x4 ld16(rsrc_t r, unsigned v, unsigned s, int aux) { return aux == NT ? __builtin_amdgcn_raw_buffer_load_b128(r, v, s, NT) : aux == SC1 ? __builtin_amdgcn_raw_buffer_load_b128(r, v, s, SC1) : __builtin_amdgcn_raw_buffer_load_b128(r, v, s, 0); }
__device__ __forceinline__ void wg_barrier() { asm volatile("s_waitcnt lgkmcnt(0)\n\ts_barrier" ::: "memory"); }
__device__ __forceinline__ bool any(bool p) { return __builtin_amdgcn_ballot_w64(p) != 0; }
__device__ __forceinline__ unsigned now() { return (unsigned)__builtin_amdgcn_s_memrealtime(); }
__device__ __forceinline__ unsigned tagged(float v, unsigned m, unsigned t) { return ((__float_as_uint(v) + ((m + 1) >> 1)) & ~m) | (t & m); }
__device__ __forceinline__ u32x4 tagged4(f32x4 v, unsigned m, unsigned t) { return u32x4{tagged(v.x, m, t), tagged(v.y, m, t), tagged(v.z, m, t), tagged(v.w, m, t)}; }
__device__ __forceinline__ unsigned mism(u32x4 v, unsigned t) { return ((v.x ^ t) | (v.y ^ t)) | ((v.z ^ t) | (v.w ^ t)); }
__device__ __forceinline__ f32x4 untag(u32x4 v, unsigned m) { return __builtin_bit_cast(f32x4, v & ~m); }
#define CLOB() asm volatile("" ::: "memory")
#define TOUCH(x) asm volatile("" :: "v"(x) : "memory")
#define PIN(x) asm volatile("" : "+v"(x))
#define FENCE() __builtin_amdgcn_sched_barrier(0)
#define LDSW(off) (*(volatile __attribute__((address_space(3))) unsigned*)(lds + (off)))
constexpr int brev4(int t) { return ((t & 1) << 3) | ((t & 2) << 1) | ((t & 4) >> 1) | ((t & 8) >> 3); }
constexpr float LOG2E = 1.4426950408889634f;
__device__ __forceinline__ void lds_wait(unsigned char* lds, int off, unsigned want) {
  int spins = 0;
  while (LDSW(off) != want && spins < (SPIN_CAP << 4)) { ++spins; __builtin_amdgcn_s_sleep(1); }
  CLOB();
}
__device__ __forceinline__ void lds_add(unsigned char* lds, int off, unsigned v) { CLOB(); __hip_atomic_fetch_add((unsigned*)(lds + off), v, __ATOMIC_RELAXED, __HIP_MEMORY_SCOPE_WORKGROUP); CLOB(); }
template <int DUE> __device__ __forceinline__ void pace(unsigned vzero) {
  unsigned tv;
  asm volatile("ds_read_b32 %0, %1 offset:%c2\n"
               "P_%=:\n\ts_memrealtime vcc\n\ts_waitcnt lgkmcnt(0)\n\tv_readfirstlane_b32 vcc_hi, %0\n\ts_sub_i32 vcc_lo, vcc_lo, vcc_hi\n\t"
               "s_cmpk_lt_i32 vcc_lo, %c3\n\ts_cbranch_scc1 P_%="
               : "=&v"(tv) : "v"(vzero), "i"(L_TB), "i"(DUE) : "vcc", "scc", "memory");
}

#define DIAG 0
#define PROF 0
#define PSTAMP(n) do {} while (0)
#define STAMP(k) do { if constexpr ((k) == PROBE) { FENCE(); tprobe = __builtin_amdgcn_s_memrealtime(); FENCE(); } } while (0)
#define STAMP_EXIT() do { if (PSEL) while (__builtin_amdgcn_s_memrealtime() < tprobe + 6000ull) __builtin_amdgcn_s_sleep(2); } while (0)
#define STAMP_DECL u64 tprobe = 0
#define STAMP_VAL(k, v) do {} while (0)
#define TUNE_DECL do {} while (0)
#define TUNE1 0
enum { A_X, A_WQ, A_WG, A_WU, A_K, A_WS, A_V, A_WO, A_WD, A_OUT };
#define DESC(n) const void* p##n, unsigned n##n, unsigned f##n
__global__ void __launch_bounds__(576, 3)
fused_decode(DESC(x), DESC(wq), DESC(wg), DESC(wu), DESC(k), DESC(ws), DESC(v), DESC(wo), DESC(wd), DESC(out)) {
  __shared__ __attribute__((aligned(16))) unsigned char lds[L_END];
  const auto* ka = (const __attribute__((address_space(4))) rsrc_t*)__builtin_amdgcn_kernarg_segment_ptr();
  int tid = threadIdx.x, bid = blockIdx.x;
  STAMP_DECL;
  { const rsrc_t rt = ka[PX < 0 ? 0 : PX];
    unsigned d = PX < 0 ? 0u : __builtin_amdgcn_raw_buffer_load_b32(rt, (tid >> 6) == 1 ? 0u : OOB, bid * PSTR, 0);
    TOUCH(d); FENCE(); tprobe = __builtin_amdgcn_s_memrealtime(); asm volatile("s_waitcnt lgkmcnt(0)\n s_barrier" ::: "memory"); FENCE(); }
  rsrc_t rX, rP, rWO, rWS; u64 t0; int wv, h, par, span, ps1, ps2, ps3; unsigned poff, pfoff, ep, cpf; u32x4 x0, x1;
  { u64 pc; int t1, t2, t3, t4;
    asm volatile(
      "s_load_dwordx4 %[rX], %[ka], 0x0\n\t"
      "s_load_dwordx4 %[rWO], %[ka], 0x70\n\t"
      "s_load_dwordx4 %[rWS], %[ka], 0x50\n\t"
      "s_memrealtime %[t0]\n\t"
      "v_readfirstlane_b32 %[wv], %[tid]\n\t"
      "s_and_b32 %[t1], %[bid], 7\n\t"
      "s_lshr_b32 %[t2], %[bid], 6\n\t"
      "s_lshl2_add_u32 %[h], %[t1], %[t2]\n\t"
      "s_lshr_b32 %[wv], %[wv], 6\n\t"
      "s_cbranch_scc1 S_%=\n\t"
      "s_setprio 3\n\t"
      "s_getpc_b64 %[pc]\n\t"
      "s_lshr_b32 %[t1], %[bid], 3\n\t"
      "v_lshl_add_u32 %[poff], %[t1], 2, %[tid]\n\t"
      "v_min_u32 %[poff], %c[cl], %[poff]\n\t"
      "v_lshlrev_b32 %[poff], 7, %[poff]\n\t"
      "s_mov_b64 exec, 15\n\t"
      "global_load_dword %[cpf], %[poff], %[pc]\n\t"
      "s_mov_b64 exec, -1\n\t"
      "v_mov_b32 %[poff], 0\n\t"
      "v_lshlrev_b32 %[pfoff], 2, %[tid]\n\t"
      "ds_write_b32 %[pfoff], %[poff] offset:%c[lf]\n\t"
      "s_waitcnt lgkmcnt(0)\n\t"
      "v_lshlrev_b32 %[poff], 5, %[tid]\n\t"
      "s_buffer_load_dword %[ep], %[rWS], 0x0\n\t"
      "buffer_load_dwordx4 %[x0], %[poff], %[rX], 0 offen\n\t"
      "buffer_load_dwordx4 %[x1], %[poff], %[rX], 0 offen offset:16\n\t"
      "s_barrier\n\t"
      "s_branch E_%=\n"
      "S_%=:\n\t"
      "s_cmp_lt_u32 %[wv], 5\n\t"
      "s_cbranch_scc1 Q_%=\n\t"
      "s_cmpk_ge_u32 %[bid], 176\n\t"
      "s_cselect_b32 %[ps3], 0x30, 0x20\n\t"
      "s_cselect_b32 %[ps1], 11, 0\n\t"
      "s_lshr_b32 %[t4], %[bid], 4\n\t"
      "s_sub_u32 %[t4], %[t4], %[ps1]\n\t"
      "s_bfe_u32 %[t3], %[bid], 0x20003\n\t"
      "s_lshl2_add_u32 %[t2], %[t2], %[t3]\n\t"
      "s_add_u32 %[t2], %[t2], 5\n\t"
      "s_bfe_u32 %[ps1], %[bid], 0x10005\n\t"
      "s_lshl1_add_u32 %[t1], %[t1], %[ps1]\n\t"
      "s_cmp_ge_u32 %[wv], 7\n\t"
      "s_cselect_b32 %[ps3], 0x30, %[ps3]\n\t"
      "s_cselect_b32 %[t4], %[t2], %[t4]\n\t"
      "s_cselect_b32 %[t1], %[t1], %[bid]\n\t"
      "s_load_dwordx4 %[rP], %[ka], %[ps3]\n\t"
      "s_and_b32 %[par], %[t1], 1\n\t"
      "s_and_b32 %[span], %[t1], 14\n\t"
      "s_andn2_b32 %[t1], 1, %[wv]\n\t"
      "s_or_b32 %[span], %[span], %[t1]\n\t"
      "s_lshl_b32 %[t4], %[t4], 2\n\t"
      "s_sub_u32 %[t4], %[t4], %[par]\n\t"
      "s_lshl_b32 %[t4], %[t4], 6\n\t"
      "s_movk_i32 %[t2], 2752\n\t"
      "s_branch J_%=\n"
      "Q_%=:\n\t"
      "s_load_dwordx4 %[rP], %[ka], 0x10\n\t"
      "s_bfe_u32 %[par], %[bid], 0x10003\n\t"
      "s_lshr_b32 %[t1], %[bid], 2\n\t"
      "s_and_b32 %[t1], %[t1], 12\n\t"
      "s_add_u32 %[span], %[t1], %[wv]\n\t"
      "s_sub_u32 %[span], %[span], 1\n\t"
      "s_lshl_b32 %[t4], %[h], 8\n\t"
      "s_movk_i32 %[t2], 8192\n"
      "J_%=:\n\t"
      "v_lshrrev_b32 %[poff], 1, %[tid]\n\t"
      "s_lshl_b32 %[t1], %[span], 5\n\t"
      "v_and_b32 %[pfoff], 15, %[tid]\n\t"
      "s_or_b32 %[t1], %[t1], %[par]\n\t"
      "v_lshl_add_u32 %[pfoff], %[pfoff], 4, %[t4]\n\t"
      "v_and_or_b32 %[poff], %[poff], 24, %[t1]\n\t"
      "s_bfe_u32 %[t1], %[bid], 0x30003\n\t"
      "v_cmp_gt_u32 vcc, %[t2], %[pfoff]\n\t"
      "s_lshl3_add_u32 %[t1], %[h], %[t1]\n\t"
      "v_mad_u32_u24 %[poff], %[poff], %[t2], %[pfoff]\n\t"
      "v_mov_b32 %[pfoff], 0x7ff00000\n\t"
      "s_lshl_b32 %[t1], %[t1], 14\n\t"
      "v_cndmask_b32 %[poff], %[pfoff], %[poff], vcc\n\t"
      "s_sub_u32 %[t1], %[t1], 0x400\n\t"
      "s_lshl_b32 %[ps1], %[t2], 1\n\t"
      "v_lshl_add_u32 %[pfoff], %[tid], 4, %[t1]\n\t"
      "s_lshl_b32 %[ps2], %[t2], 2\n\t"
      "s_waitcnt lgkmcnt(0)\n\t"
      "s_mul_i32 %[ps3], %[t2], 6\n"
      "E_%=:"
      : [rX] "=&s"(rX), [rP] "=&s"(rP), [rWO] "=&s"(rWO), [rWS] "=&s"(rWS), [t0] "=&s"(t0), [wv] "=&s"(wv), [h] "=&s"(h), [par] "=&s"(par), [span] "=&s"(span),
        [ps1] "=&s"(ps1), [ps2] "=&s"(ps2), [ps3] "=&s"(ps3), [poff] "=&v"(poff), [pfoff] "=&v"(pfoff), [cpf] "=&v"(cpf), [x0] "=&v"(x0), [x1] "=&v"(x1), [ep] "=&s"(ep),
        [pc] "=&s"(pc), [t1] "=&s"(t1), [t2] "=&s"(t2), [t3] "=&s"(t3), [t4] "=&s"(t4)
      : [ka] "s"(ka), [bid] "s"(bid), [tid] "v"(tid), [cl] "i"(CODE_LINES - 1), [lf] "i"(L_F)
      : "vcc", "scc", "memory");
  }
  asm("" : "+s"(rX)); asm("" : "+s"(rWO)); asm("" : "+s"(rP)); asm("" : "+s"(rWS)); asm("" : "+s"(wv)); asm("" : "+s"(h)); asm("" : "+s"(par)); asm("" : "+s"(span)); asm("" : "+s"(t0));
  static_assert(WS_EPOCH == 0 && 2 * FF == 2752 && OOB == 0x7ff00000u && A_WO == 7 && A_WS == 5 && L_F + 256 <= L_END, "constants of the block above");
#define INDICES   \
  asm volatile("" : "+s"(bid), "+s"(wv)); asm volatile("" : "+v"(tid)); \
  const int lane = tid & 63, g = lane >> 4, c = lane & 15, xcd = bid & 7, j = bid >> 3; \
  const int hq = j >> 3, b = (j >> 2) & 1, s = j & 3, i16 = hq * 4 + s; \
  const int dcnt = i16 < 3 ? 4 : i16 < 6 ? 8 : i16 < 12 ? 12 : 16, dstart = xcd * 172 + dcnt * i16 - (i16 < 3 ? 0 : i16 < 6 ? 12 : i16 < 12 ? 36 : 84)

  if (wv == 0) {
    STAMP(10);
    INDICES;
    const unsigned tstart = (unsigned)t0;
    if (lane < 9) LDSW(L_TB + lane * 4) = tstart - (1u << 30);
    FENCE();
    {
      float e[16];
      asm volatile("s_waitcnt vmcnt(0)" : "+v"(x0), "+v"(x1), "+v"(cpf) :: "memory");
      STAMP(11);
      sfor<4>([&](auto ec) INL { constexpr int k = decltype(ec)::v; const unsigned u0 = x0[k], u1 = x1[k];
        e[2 * k] = bflo(u0); e[2 * k + 1] = bfhi(u0); e[8 + 2 * k] = bflo(u1); e[9 + 2 * k] = bfhi(u1); });
      float a = (((e[0] + e[1]) + (e[2] + e[3])) + ((e[4] + e[5]) + (e[6] + e[7]))) + (((e[8] + e[9]) + (e[10] + e[11])) + ((e[12] + e[13]) + (e[14] + e[15])));
      a = row_sum(a); a = sw16(a, a) * (1.f / 512.f);
      float v0 = 0.f, v1 = 0.f;
      sfor<8>([&](auto ec) INL { constexpr int k = decltype(ec)::v; const float d0 = e[k] - a, d1 = e[8 + k] - a; v0 = fmaf(d0, d0, v0); v1 = fmaf(d1, d1, v1); });
      float v = row_sum(v0 + v1); v = sw16(v, v) * (1.f / 512.f);
      const float mean = rbf(a), rden = __builtin_amdgcn_rcpf(rbf(__builtin_amdgcn_sqrtf(rbf(rbf(v) + 1e-5f))));
      u32x4 n0, n1;
      sfor<4>([&](auto ec) INL { constexpr int k = decltype(ec)::v;
        const unsigned d0 = pkbf(e[2 * k] - mean, e[2 * k + 1] - mean), d1 = pkbf(e[8 + 2 * k] - mean, e[9 + 2 * k] - mean);
        n0[k] = pkbf(bflo(d0) * rden, bfhi(d0) * rden); n1[k] = pkbf(bflo(d1) * rden, bfhi(d1) * rden); });
      *(u32x4*)(lds + L_XN + lane * 32) = n0; *(u32x4*)(lds + L_XN + lane * 32 + 16) = n1;
    }
    CLOB(); LDSW(F_XN) = 1u;
    STAMP(7);
    FENCE();
    asm volatile("s_waitcnt lgkmcnt(0)" : "+s"(ep), "+s"(par) :: "memory");
    const rsrc_t rV = ka[A_V], rOut = ka[A_OUT];
    unsigned ooff = (lane < 16 && s == 0) ? lane * 2 : OOB;
    const unsigned xr = __builtin_amdgcn_raw_buffer_load_b32(rX, ooff & ~2u, b * 1024 + h * 32, 0);
    const unsigned tl0 = __builtin_amdgcn_raw_buffer_load_b32(rV, 0, ((b * 32 + h) * 4 + s) << 18, NT), tl1 = __builtin_amdgcn_raw_buffer_load_b32(rOut, ooff & ~2u, b * 1024 + h * 32, 0);
    const unsigned tag = ((ep * 747796405u + 2891336453u) & ~0xfu) | ((ep + 1u) & 0xfu);
    LDSW(L_TAG) = tag;
    const int mA = bid >= 176, cA = (bid >> 4) - 11 * mA, sA = bid & 15, cB = 5 + i16, sB = 2 * xcd + b;
    const unsigned dQ = WS_QX + (h * 8 + (j & 7)) * 1024, dA = WS_FX + (mA * 16 + sA) * (2 * FROW * 4) + (cA * 128 + 32 - 32 * (sA & 1)) * 4,
                   dB = WS_FX + (16 + sB) * (2 * FROW * 4) + (cB * 128 + 32 - 32 * (sB & 1)) * 4;
    unsigned fvo = (lane & 31) * 16 + (lane >> 5) * (FROW * 4), qo = (lane >> 5) * 1024 + (lane & 31) * 16;
    const bool fon = 4 * (lane & 3) < dcnt;
    unsigned fo = fon ? (lane >> 2) * (2 * FROW * 4) + (lane & 3) * 16 : OOB, fmask = fon ? 0xffu : 0u;
    const unsigned fbase = WS_FX + b * (FROW * 4) + (32 + dstart) * 4;
    PIN(fvo); PIN(qo); PIN(fo); PIN(fmask);
    lds_wait(lds, F_PL, 8);
    STAMP(1);
    { const unsigned char* src = lds + L_PL + lane * 16;
      const f32x4 vq = (*(const f32x4*)src + *(const f32x4*)(src + 1024)) + (*(const f32x4*)(src + 2048) + *(const f32x4*)(src + 3072));
      const f32x4 vA = *(const f32x4*)(src + 4096) + *(const f32x4*)(src + 5120), vB = *(const f32x4*)(src + 6144) + *(const f32x4*)(src + 7168);
      __builtin_amdgcn_raw_buffer_store_b128(tagged4(vq, 3u, tag), rWS, lane * 16, dQ, SC1);
      __builtin_amdgcn_raw_buffer_store_b128(tagged4(vA, 0xffu, tag), rWS, fvo, dA, SC1);
      __builtin_amdgcn_raw_buffer_store_b128(tagged4(vB, 0xffu, tag), rWS, i16 < 6 ? fvo : OOB, dB, SC1);
    }
    asm volatile("s_waitcnt vmcnt(0)" ::: "memory");
    { u32x4 qp[4], fg, fu; int spins = 0;
      _Pragma("unroll 1") do {
        CLOB();
        sfor<4>([&](auto kc) INL { constexpr int k = decltype(kc)::v; qp[k] = ld16(rWS, qo, WS_QX + h * 8192 + b * 512 + k * 2048, SC1); });
        fg = ld16(rWS, fo, fbase, SC1); fu = ld16(rWS, fo, fbase + 16 * 2 * FROW * 4, SC1);
      } while (any((((mism(qp[0], tag) | mism(qp[1], tag) | mism(qp[2], tag) | mism(qp[3], tag)) & 3u) | ((mism(fg, tag) | mism(fu, tag)) & fmask)) != 0) && ++spins < SPIN_CAP);
      STAMP_VAL(12, spins);
      const f32x4 qs = (untag(qp[0], 3u) + untag(qp[1], 3u)) + (untag(qp[2], 3u) + untag(qp[3], 3u));
      *(u32x2*)(lds + L_Q + (lane & 31) * 8) = u32x2{pkbf(sw32(qs.x, qs.x), sw32(qs.y, qs.y)), pkbf(sw32(qs.z, qs.z), sw32(qs.w, qs.w))};
      float gv = tr4(untag(fg, 0xffu)), uv = tr4(untag(fu, 0xffu));
      gv += dppf<ROR8>(gv); uv += dppf<ROR8>(uv); gv += dppf<ROR4>(gv); uv += dppf<ROR4>(uv);
      gv = rbf(gv); uv = rbf(uv);
      const float z = 0.7978845608028654f * (gv + 0.044715f * gv * gv * gv);
      const float ge = rbf(gv * __builtin_amdgcn_rcpf(1.f + __builtin_amdgcn_exp2f(-2.f * LOG2E * z)));
      if (c < 4) *(u16*)(lds + L_H + (4 * c + g) * 2) = (u16)pkbf(ge * uv, 0.f);
      CLOB(); LDSW(F_Q) = 1u;
    }
    STAMP(3);
    TOUCH(tl0); TOUCH(tl1); TOUCH(xr);
    { TUNE_DECL;
      lds_wait(lds, F_KM, 8);
      const unsigned tK = tstart + ((LDSW(L_T2) - 8u * tstart) >> 3);
      __builtin_amdgcn_raw_buffer_store_b64(u32x2{tK, tag}, rWS, (lane == 0 && s == hq && b == (hq & 1)) ? 0u : OOB, WS_TM + h * 8, SC1);
      for (int k = 0; k < TDELAY; ++k) __builtin_amdgcn_s_sleep(16);
      const int dn = (int)(now() - tK);
      const u32x2 ta = __builtin_amdgcn_raw_buffer_load_b64(rWS, (lane & 31) * 8, WS_TM, SC1);
      const float mt = wave_sum((float)(ta.y == tag ? (int)(ta.x - tK) : dn)) * (1.f / 64.f);
      if (lane < 9) LDSW(L_TB + lane * 4) = tK + (int)fminf(mt, 400.f) + (lane ? ((2 * lane - 9) * STAGW) >> 5 : 0) + TUNE1;
      STAMP_VAL(5, (u64)(t0 + (tK + (int)mt - tstart)));
      STAMP(2);
    }
    { const unsigned mb = WS_ML + (b * 32 + h) * 128;
      lds_wait(lds, F_ML, 8);
      const f32x2 ml = *(const f32x2*)(lds + L_ML + (lane & 7) * 8);
      float m = fmaxf(ml.x, dppf<XOR1>(ml.x)); m = fmaxf(m, dppf<XOR2>(m)); m = fmaxf(m, dppf<HMIR>(m));
      float l = ml.y * __builtin_amdgcn_exp2f((ml.x - m) * LOG2E); l += dppf<XOR1>(l); l += dppf<XOR2>(l); l += dppf<HMIR>(l);
      __builtin_amdgcn_raw_buffer_store_b128(u32x4{__float_as_uint(m), tag, __float_as_uint(l), tag}, rWS, lane == 0 ? 0u : OOB, mb + s * 16, SC1);
      __builtin_amdgcn_s_sleep(127);
      u32x4 mp; int spins = 0;
      _Pragma("unroll 1") do {
        __builtin_amdgcn_s_sleep(8); CLOB();
        mp = ld16(rWS, (lane & 3) * 16, mb, SC1);
      } while (any(mp.y != tag || mp.w != tag) && ++spins < SPIN_CAP);
      const float mj = __uint_as_float(mp.x), lj = __uint_as_float(mp.z);
      float M = fmaxf(mj, dppf<XOR1>(mj)); M = fmaxf(M, dppf<XOR2>(M));
      float L = lj * __builtin_amdgcn_exp2f((mj - M) * LOG2E); L += dppf<XOR1>(L); L += dppf<XOR2>(L);
      *(f32x2*)(lds + L_MR) = f32x2{M, __builtin_amdgcn_rcpf(L)};
      CLOB(); LDSW(F_MR) = 1u;
    }
    STAMP(4);
    unsigned rrd = L_RL + c * 32; float xf = (lane & 1) ? bfhi(xr) : bflo(xr);
    PIN(rrd); PIN(xf); PIN(ooff);
    wg_barrier();
    STAMP(6);
    if (s != 0) { STAMP_EXIT(); return; }
    wg_barrier();
    STAMP(8);
    const f32x4 r4 = *(const f32x4*)(lds + rrd) + *(const f32x4*)(lds + rrd + 16);
    __builtin_amdgcn_raw_buffer_store_b16((u16)pkbf(xf + ((r4.x + r4.y) + (r4.z + r4.w)), 0.f), rOut, ooff, b * 1024 + h * 32, 0);
    if (bid == 0) __builtin_amdgcn_raw_buffer_store_b32(tag, rWS, lane == 0 ? 0u : OOB, WS_EPOCH, 0);
    STAMP(9);
    STAMP_EXIT();
    return;
  }

  u32x4 P[4], PF[3], KV[64];
  P[0] = ld16(rP, poff, 0, NT); P[1] = ld16(rP, poff, ps1, NT); P[2] = ld16(rP, poff, ps2, NT); P[3] = ld16(rP, poff, ps3, NT);
  asm volatile("s_barrier" ::: "memory");
  PF[0] = ld16(rWO, pfoff, 0, 0); PF[1] = ld16(rWO, pfoff, 8192, 0);
  CLOB(); FENCE();
  STAMP(10);
  const rsrc_t rK = ka[A_K], rV = ka[A_V], rWD = ka[A_WD];
  INDICES;
  const int w = wv - 1, rot = (bid << 12) & 0x38000;
  unsigned kvoff = (((b * 32 + h) * 4 + s) << 18) + (tid - 64) * 16; PIN(kvoff);
  u32x4 sel, q;
  u32x2 WO[32], WD[4];
  u32x8 BO[8];
  unsigned wooff = 0, wdoff = 0, owr = 0, ord = 0, yst = 0, yld = 0, rwr = 0, ytag = 0, vzero = 0, vw4 = wv * 4;
  int sidx = 0;
  f32x4 oacc = {0.f, 0.f, 0.f, 0.f}, yacc[4];
  float t1 = 0.f, t2 = 0.f, t3 = 0.f, t4 = 0.f, sc[2] = {0.f, 0.f}, mw = 0.f, cw = 0.f;
  unsigned pp[2] = {0u, 0u};
  auto mkA = [&](unsigned src) INL -> u32x4 { u32x4 a; a.x = __builtin_amdgcn_perm(src, src, sel.x); a.y = __builtin_amdgcn_perm(src, src, sel.y);
                                              a.z = __builtin_amdgcn_perm(src, src, sel.z); a.w = __builtin_amdgcn_perm(src, src, sel.w); return a; };
  auto tile_off = [&](int t) INL { int r = rot; asm volatile("" : "+s"(r)); return (r + t * 8192) & 0x3ffff; };

  auto consumeK = [&](auto tc) INL {
    constexpr int t = decltype(tc)::v;
    const u32x4 k = KV[t];
    float d = dot2(k.x, q.x, 0.f); d = dot2(k.y, q.y, d); d = dot2(k.z, q.z, d); d = dot2(k.w, q.w, d);
    if constexpr ((t & 1) == 0) { t1 = d; PIN(t1); }
    else {
      { const float keep = (c & 8) ? d : t1, send = (c & 8) ? t1 : d; d = keep + dppf<ROR8>(send); }
      if constexpr ((t & 2) == 0) { t2 = d; PIN(t2); }
      else {
        { const float keep = (c & 4) ? d : t2, send = (c & 4) ? t2 : d; d = keep + dppf<HMIR>(send); }
        if constexpr ((t & 4) == 0) { t3 = d; PIN(t3); }
        else {
          { const float keep = (c & 2) ? d : t3, send = (c & 2) ? t3 : d; d = keep + dppf<XOR2>(send); }
          if constexpr ((t & 8) == 0) { t4 = d; PIN(t4); }
          else { const float keep = (c & 1) ? d : t4, send = (c & 1) ? t4 : d; sc[t >> 4] = keep + dppf<XOR1>(send); PIN(sc[t >> 4]); }
        }
      }
    }
    if constexpr (t == 31) {
      const float s0 = rbf(rbf(sc[0]) * 0.08838834764831845f), s1 = rbf(rbf(sc[1]) * 0.08838834764831845f);
      mw = wave_max(fmaxf(s0, s1));
      const float p0 = __builtin_amdgcn_exp2f((s0 - mw) * LOG2E), p1 = __builtin_amdgcn_exp2f((s1 - mw) * LOG2E);
      const float lw = wave_sum(p0 + p1);
      pp[0] = pkbf(p0, p0 - rbf(p0)); pp[1] = pkbf(p1, p1 - rbf(p1)); PIN(pp[0]); PIN(pp[1]);
      if (lane == 0) { *(f32x2*)(lds + L_ML + w * 8) = f32x2{mw, lw}; lds_add(lds, F_ML, 1u); }
      STAMP(4);
    }
  };

  constexpr Sched S{};
  sfor<S.n>([&](auto nc) INL {
    constexpr Act A = S.a[decltype(nc)::v];
    constexpr int i = A.i;
    if constexpr (A.issue) {
      if constexpr (A.t == T_K || A.t == T_V) {
        constexpr int n = i + (A.t == T_V ? 32 : 0);
        if constexpr (n >= APACE0) { pace<(AEND - (63 - n) * ATAU) / 16>(vw4); FENCE(); }
        KV[n] = ld16(A.t == T_V ? rV : rK, kvoff, tile_off(i), NT);
      }
      else if constexpr (A.t == T_PF && i == 2) PF[2] = ld16(rWD, 2 * w < dcnt ? (tid - 64) * 16 : OOB, (dstart + b * (dcnt >> 1)) * 1024, 0);
      else if constexpr (A.t == T_WO) WO[i] = __builtin_amdgcn_raw_buffer_load_b64(rWO, wooff, ((i >> 3) * 32 + (i & 7)) * 1024, NT);
      else if constexpr (A.t == T_WD) WD[i] = __builtin_amdgcn_raw_buffer_load_b64(rWD, wdoff, i * 1024, NT);
    } else {
      if constexpr (A.t == T_P) {
        if constexpr (i == 0) {
          const unsigned sb = (c & 8) ? 0x0302u : 0x0100u, one = (c & 1) ? ((sb << 16) | 0x0c0cu) : (0x0c0c0000u | sb); const int v = (c & 7) >> 1;
          sel.x = v == 0 ? one : 0x0c0c0c0cu; sel.y = v == 1 ? one : 0x0c0c0c0cu; sel.z = v == 2 ? one : 0x0c0c0c0cu; sel.w = v == 3 ? one : 0x0c0c0c0cu;
          wooff = h * 131072 + g * 8192 + w * 128 + c * 8;
          wdoff = (4 * g < dcnt) ? (unsigned)(dstart * 1024 + g * 4096 + w * 128 + c * 8) : OOB;
          owr = L_OL + (w + (lane >= 32 ? 8 : 0)) * OL_STR + c * 8 + (g & 1) * 4;
          ord = L_OL + c * OL_STR + g * 16;
          sidx = (c & 8) ? 0xeeee : 0x4444;
          yst = g ? OOB : WS_Y + (b * 128 + h * 4 + s) * 2048 + w * 256 + c * 16;
          yld = WS_Y + b * 128 * 2048 + h * 64 + ((tid - 64) >> 2) * 2048 + (lane & 3) * 16;
          rwr = c < 4 ? L_RL + (4 * c + g) * 32 + w * 4 : L_PL + lane * 4;
          PIN(sel); PIN(wooff); PIN(wdoff); PIN(owr); PIN(ord); PIN(sidx); PIN(yst); PIN(yld); PIN(rwr); PIN(vzero); PIN(vw4);
        }
        TOUCH(P[i]);
        if constexpr (i == 3) {
          STAMP(9);
          lds_wait(lds, F_XN, 1u);
          STAMP(11);
          const u32x4 xa = *(const u32x4*)(lds + L_XN + (c >> 3) * 1024 + g * 16 + span * 64);
          f32x4 pacc = {0.f, 0.f, 0.f, 0.f};
          sfor<4>([&](auto ec) INL { constexpr int e = decltype(ec)::v; const unsigned u = xa[e];
            pacc = mfma32(mkA(__builtin_amdgcn_perm(u, u, par ? 0x03020302u : 0x01000100u)), P[e], pacc); });
          *(f32x4*)(lds + L_PL + w * 1024 + (g >> 1) * 512 + c * 32 + (g & 1) * 16) = pacc;
          if (lane == 0) lds_add(lds, F_PL, 1u);
          STAMP(1);
        }
      } else if constexpr (A.t == T_PF) { TOUCH(PF[i]);
      } else if constexpr (A.t == T_K) {
        TOUCH(KV[i]);
        PSTAMP(i);
        if constexpr (i == KM) { const unsigned t = now(); if (lane == 0) { lds_add(lds, L_T2, t); lds_add(lds, F_KM, 1u); } }
        if constexpr (i == QPOS) {
          STAMP(2);
          lds_wait(lds, F_Q, 1u);
          STAMP(3);
          q = *(const u32x4*)(lds + L_Q + c * 16);
          ytag = LDSW(L_TAG) & 0xfu;
          PIN(ytag);
        }
      } else if constexpr (A.t == T_WD) {
        TOUCH(WD[i]);
        if constexpr (i == 3) {
          u32x2 hA = *(const u32x2*)(lds + L_H + g * 8);
          if (c != 0) hA = u32x2{0u, 0u};
          sfor<4>([&](auto tc) INL { constexpr int t = decltype(tc)::v; constexpr unsigned ps = (t & 1) ? 0x07060302u : 0x05040100u;
            const unsigned w0 = WD[0][t >> 1], w1 = WD[1][t >> 1], w2 = WD[2][t >> 1], w3 = WD[3][t >> 1];
            const u32x2 bb = {__builtin_amdgcn_perm(w1, w0, ps), __builtin_amdgcn_perm(w3, w2, ps)};
            yacc[t] = mfma16(hA, bb, f32x4{0.f, 0.f, 0.f, 0.f}); PIN(yacc[t]); });
        }
      } else if constexpr (A.t == T_WO) {
        TOUCH(WO[i]);
        if constexpr ((i & 7) == 7) {
          constexpr int u = i >> 3;
          sfor<4>([&](auto tc) INL { constexpr int t = decltype(tc)::v; constexpr unsigned ps = (t & 1) ? 0x07060302u : 0x05040100u;
            sfor<4>([&](auto vc) INL { constexpr int v = decltype(vc)::v;
              const unsigned lo = WO[u * 8 + 2 * v][t >> 1], hi = WO[u * 8 + 2 * v + 1][t >> 1];
              BO[(u >> 1) * 4 + t][(u & 1) * 4 + v] = __builtin_amdgcn_perm(hi, lo, ps); });
            PIN(BO[(u >> 1) * 4 + t]); });
        }
      } else if constexpr (A.t == T_V) {
        oacc = mfma32(mkA(dppu<BCAST + brev4(i & 15)>(pp[i >> 4])), KV[32 + i], oacc); PIN(oacc);
        PSTAMP(32 + i);
        if constexpr (i == 27) {
          lds_wait(lds, F_MR, 1u);
          const f32x2 mr = *(const f32x2*)(lds + L_MR);
          cw = __builtin_amdgcn_exp2f((mw - mr.x) * LOG2E) * mr.y;
          PIN(cw);
        }
      }
      sfor<A.k1 - A.k0>([&](auto kc) INL { FENCE(); consumeK(IC<A.k0 + decltype(kc)::v>{}); });
    }
    FENCE();
  });

  STAMP(5);
  *(unsigned*)(lds + owr) = pkbf(sw32(oacc.x, oacc.z) * cw, sw32(oacc.y, oacc.w) * cw);
  wg_barrier();
  STAMP(6);
  { const u32x4 a0 = *(const u32x4*)(lds + ord), a1 = *(const u32x4*)(lds + ord + 64);
    FENCE();
    auto sum4 = [&](f32x4 u) INL { const f32x2 p = f32x2{u.x, u.y} + f32x2{u.z, u.w}; return p.x + p.y; };
    auto fin = [&](float y) INL { y = sw16(y, y); const unsigned t = ((__float_as_uint(y) + 8u) & ~0xfu) | ytag; return __builtin_amdgcn_permlane32_swap(t, t, false, false); };
    yacc[0] = smfma64(a0, BO[0], yacc[0], sidx); yacc[2] = smfma64(a0, BO[2], yacc[2], sidx); FENCE();
    yacc[0] = smfma64(a1, BO[4], yacc[0], sidx); yacc[2] = smfma64(a1, BO[6], yacc[2], sidx); FENCE();
    yacc[1] = smfma64(a0, BO[1], yacc[1], sidx); FENCE();
    float z0 = sum4(yacc[0]); PIN(z0); FENCE();
    yacc[3] = smfma64(a0, BO[3], yacc[3], sidx); FENCE();
    float z2 = sum4(yacc[2]); PIN(z2); FENCE();
    yacc[1] = smfma64(a1, BO[5], yacc[1], sidx); FENCE();
    float ya = sw32(z0, z2); PIN(ya); FENCE();
    yacc[3] = smfma64(a1, BO[7], yacc[3], sidx); FENCE();
    const auto sa = fin(ya);
    const auto sb = fin(sw32(sum4(yacc[1]), sum4(yacc[3])));
    __builtin_amdgcn_raw_buffer_store_b128(u32x4{sa[0], sb[0], sa[1], sb[1]}, rWS, yst, 0, SC1);
  }
  STAMP(7);
  if (s != 0) { STAMP_EXIT(); return; }
  { u32x4 y; int spins = SPIN_CAP;
    pace<AEND / 16 + YPOLL>(vzero);
    unsigned t0_, t1_;
    asm volatile("s_waitcnt vmcnt(0)\n"
                 "L_%=:\n\tbuffer_load_dwordx4 v[2:5], %[off], %[rs], 0 offen sc1\n\ts_waitcnt vmcnt(0)\n\t"
                 "v_bitop3_b32 %[t0], v2, v3, %[tag] bitop3:0x7e\n\tv_bitop3_b32 %[t1], v4, v5, %[tag] bitop3:0x7e\n\t"
                 "v_bitop3_b32 %[t0], %[t0], %[t1], 15 bitop3:0xa8\n\tv_cmp_ne_u32 vcc, 0, %[t0]\n\ts_cbranch_vccz D_%=\n\t"
                 "s_sub_u32 %[sp], %[sp], 1\n\ts_cbranch_scc0 L_%=\n"
                 "D_%=:"
                 : "=&{v[2:5]}"(y), [t0] "=&v"(t0_), [t1] "=&v"(t1_), [sp] "+s"(spins) : [off] "v"(yld), [rs] "s"(rWS), [tag] "v"(ytag) : "vcc", "scc", "memory");
    STAMP(8);
    float v = tr4(untag(y, 0xfu)); v += dppf<ROR8>(v); v += dppf<ROR4>(v);
    *(float*)(lds + rwr) = v;
    wg_barrier();
  }
  STAMP_EXIT();
}
}

extern "C" void kernel_launch(void* const* d_in, const int* in_sizes, int n_in, void* d_out, int out_size, void* d_ws, size_t ws_size, hipStream_t stream) {
  const unsigned F = 0x00020000u;
  fused_decode<<<256, 576, 0, stream>>>(d_in[0], 2048u, F, d_in[3], 512u * 8192u, F, d_in[6], 512u * 2u * FF, F, d_in[7], 512u * 2u * FF, F, d_in[1], 64u << 20, F,
                                        d_ws, WS_END, F, d_in[2], 64u << 20, F, d_in[4], 4096u * 1024u, F, d_in[8], FF * 1024u, F, d_out, 2048u, F);
}
```

```cpp
#include <hip/hip_runtime.h>
#include <stdint.h>
#include <utility>

#define CODE_LINES 113
#define WLIM 40
#define QPOS 14
#define WO0 3
#define WO1 59
#define WD_POS 22
#define KM 16
#define TDELAY 5
#define APACE0 29
#define ATAU 450
#define AEND 19650
#define STAGW 56
#define YPOLL 125

namespace {
typedef unsigned u32x8 __attribute__((ext_vector_type(8)));
typedef unsigned u32x4 __attribute__((ext_vector_type(4)));
typedef unsigned u32x2 __attribute__((ext_vector_type(2)));
typedef float f32x4 __attribute__((ext_vector_type(4)));
typedef float f32x2 __attribute__((ext_vector_type(2)));
typedef __bf16 bf16x16 __attribute__((ext_vector_type(16)));
typedef __bf16 bf16x8 __attribute__((ext_vector_type(8)));
typedef __bf16 bf16x2 __attribute__((ext_vector_type(2)));
typedef short s16x4 __attribute__((ext_vector_type(4)));
typedef unsigned short u16;
typedef unsigned long long u64;
typedef __amdgpu_buffer_rsrc_t rsrc_t;

#define INL __attribute__((always_inline))
template <int I> struct IC { static constexpr int v = I; };
template <class F, int... Is> __device__ __forceinline__ void sfor_impl(F&& f, std::integer_sequence<int, Is...>) { (f(IC<Is>{}), ...); }
template <int N, class F> __device__ __forceinline__ void sfor(F&& f) { sfor_impl(f, std::make_integer_sequence<int, N>{}); }

constexpr int NT = 2, SC1 = 16;
constexpr int FF = 1376, FROW = 1440;
constexpr unsigned WS_EPOCH = 0;
constexpr unsigned WS_ML = 4096;
constexpr unsigned WS_TM = WS_ML + 64 * 128;
constexpr unsigned WS_QX = WS_TM + 2048;
constexpr unsigned WS_FX = WS_QX + 32 * 8 * 1024;
constexpr unsigned WS_Y = 1u << 20;
static_assert(WS_FX + 2 * 2 * 8 * FROW * 4 <= WS_Y, "ws map");
constexpr unsigned WS_END = WS_Y + 2 * 128 * 2048;
constexpr unsigned OOB = 0x7ff00000u;
constexpr int SPIN_CAP = 1 << 18;
constexpr int L_PL = 0, L_XN = 8192, L_Q = L_XN + 2048, L_H = L_Q + 256, L_ML = L_H + 32, L_MR = L_ML + 64, L_F = L_MR + 16,
              F_PL = L_F, F_Q = L_F + 4, F_ML = L_F + 8, F_MR = L_F + 12, F_KM = L_F + 16, F_XN = L_F + 20, L_T2 = L_F + 28,
              L_TAG = L_F + 32, L_TB = L_F + 44, L_RL = L_F + 80, L_OL = L_RL + 512, OL_STR = 144, L_END = L_OL + 16 * OL_STR;

enum { T_P, T_PF, T_K, T_V, T_WO, T_WD };
struct Item { int t, i; };
struct Act { int issue; int t, i; int k0, k1; };
constexpr int weight(int t) { return (t == T_WO || t == T_WD) ? 4 : 8; }
struct Sched {
  Act a[240]; int n;
  constexpr Sched() : a{}, n(0) {
    Item it[120] = {}; int ni = 0;
    for (int i = 0; i < 4; ++i) it[ni++] = Item{T_P, i};
    for (int i = 0; i < 2; ++i) it[ni++] = Item{T_PF, i};
    int wo = 0;
    for (int k = 0; k < 64; ++k) {
      it[ni++] = k < 32 ? Item{T_K, k} : Item{T_V, k - 32};
      if (k >= WD_POS && k < WD_POS + 4) it[ni++] = Item{T_WD, k - WD_POS};
      while (wo < 32 && k >= WO0 && wo * (WO1 - WO0) < (k - WO0 + 1) * 32) it[ni++] = Item{T_WO, wo++};
    }
    int head = 0, fl = 0, kdone = 0, karr = 0;
    for (int j = 0; j <= ni; ++j) {
      while (head < j && (j == ni || fl + weight(it[j].t) > (head < 4 ? 48 : WLIM))) {
        Item r = it[head++]; fl -= weight(r.t);
        if (r.t == T_K) karr = r.i + 1;
        int k0 = kdone;
        if (karr > QPOS) kdone = (karr == 32 || kdone + 5 >= karr) ? karr : kdone + 5;
        a[n++] = Act{0, r.t, r.i, k0, kdone};
      }
      if (j < ni) { a[n++] = Act{1, it[j].t, it[j].i, 0, 0}; fl += weight(it[j].t); }
    }
  }
};

static_assert(WD_POS > QPOS, "h is written with q");

__device__ __forceinline__ float bflo(unsigned u) { return __uint_as_float(u << 16); }
__device__ __forceinline__ float bfhi(unsigned u) { return __uint_as_float(u & 0xffff0000u); }
__device__ __forceinline__ unsigned pkbf(float lo, float hi) { f32x2 f = {lo, hi}; bf16x2 h = __builtin_convertvector(f, bf16x2); return __builtin_bit_cast(unsigned, h); }
__device__ __forceinline__ float rbf(float f) { return bflo(pkbf(f, f)); }
__device__ __forceinline__ float dot2(unsigned a, unsigned b, float c) { return __builtin_amdgcn_fdot2_f32_bf16(__builtin_bit_cast(bf16x2, a), __builtin_bit_cast(bf16x2, b), c, false); }
template <int CTRL> __device__ __forceinline__ unsigned dppu(unsigned x) { return __builtin_amdgcn_update_dpp(0u, x, CTRL, 0xf, 0xf, true); }
template <int CTRL> __device__ __forceinline__ float dppf(float x) { return __uint_as_float(dppu<CTRL>(__float_as_uint(x))); }
constexpr int XOR1 = 0xB1, XOR2 = 0x4E, HMIR = 0x141, ROR8 = 0x128, ROR4 = 0x124, BCAST = 0x150;
__device__ __forceinline__ float row_sum(float x) { x += dppf<XOR1>(x); x += dppf<XOR2>(x); x += dppf<HMIR>(x); x += dppf<ROR8>(x); return x; }
__device__ __forceinline__ float row_max(float x) { x = fmaxf(x, dppf<XOR1>(x)); x = fmaxf(x, dppf<XOR2>(x)); x = fmaxf(x, dppf<HMIR>(x)); x = fmaxf(x, dppf<ROR8>(x)); return x; }
__device__ __forceinline__ float sw16(float a, float b) { auto s = __builtin_amdgcn_permlane16_swap(__float_as_uint(a), __float_as_uint(b), false, false); return __uint_as_float(s[0]) + __uint_as_float(s[1]); }
__device__ __forceinline__ float sw32(float a, float b) { auto s = __builtin_amdgcn_permlane32_swap(__float_as_uint(a), __float_as_uint(b), false, false); return __uint_as_float(s[0]) + __uint_as_float(s[1]); }
__device__ __forceinline__ float x16_max(float x) { auto s = __builtin_amdgcn_permlane16_swap(__float_as_uint(x), __float_as_uint(x), false, false); return fmaxf(__uint_as_float(s[0]), __uint_as_float(s[1])); }
__device__ __forceinline__ float x32_max(float x) { auto s = __builtin_amdgcn_permlane32_swap(__float_as_uint(x), __float_as_uint(x), false, false); return fmaxf(__uint_as_float(s[0]), __uint_as_float(s[1])); }
__device__ __forceinline__ float wave_sum(float x) { x = row_sum(x); x = sw16(x, x); return sw32(x, x); }
__device__ __forceinline__ float wave_max(float x) { return x32_max(x16_max(row_max(x))); }
__device__ __forceinline__ float tr4(f32x4 z) { return sw16(sw32(z.x, z.z), sw32(z.y, z.w)); }
__device__ __forceinline__ f32x4 mfma32(u32x4 a, u32x4 b, f32x4 c) { return __builtin_amdgcn_mfma_f32_16x16x32_bf16(__builtin_bit_cast(bf16x8, a), __builtin_bit_cast(bf16x8, b), c, 0, 0, 0); }
__device__ __forceinline__ f32x4 mfma16(u32x2 a, u32x2 b, f32x4 c) { return __builtin_amdgcn_mfma_f32_16x16x16bf16_1k(__builtin_bit_cast(s16x4, a), __builtin_bit_cast(s16x4, b), c, 0, 0, 0); }
__device__ __forceinline__ f32x4 smfma64(u32x4 a, u32x8 b, f32x4 c, int idx) { return __builtin_amdgcn_smfmac_f32_16x16x64_bf16(__builtin_bit_cast(bf16x8, a), __builtin_bit_cast(bf16x16, b), c, idx, 0, 0); }
__device__ __forceinline__ u32x4 ld16(rsrc_t r, unsigned v, unsigned s, int aux) { return aux == NT ? __builtin_amdgcn_raw_buffer_load_b128(r, v, s, NT) : aux == SC1 ? __builtin_amdgcn_raw_buffer_load_b128(r, v, s, SC1) : __builtin_amdgcn_raw_buffer_load_b128(r, v, s, 0); }
__device__ __forceinline__ void wg_barrier() { asm volatile("s_waitcnt lgkmcnt(0)\n\ts_barrier" ::: "memory"); }
__device__ __forceinline__ bool any(bool p) { return __builtin_amdgcn_ballot_w64(p) != 0; }
__device__ __forceinline__ unsigned now() { return (unsigned)__builtin_amdgcn_s_memrealtime(); }
__device__ __forceinline__ unsigned tagged(float v, unsigned m, unsigned t) { return ((__float_as_uint(v) + ((m + 1) >> 1)) & ~m) | (t & m); }
__device__ __forceinline__ u32x4 tagged4(f32x4 v, unsigned m, unsigned t) { return u32x4{tagged(v.x, m, t), tagged(v.y, m, t), tagged(v.z, m, t), tagged(v.w, m, t)}; }
__device__ __forceinline__ unsigned mism(u32x4 v, unsigned t) { return ((v.x ^ t) | (v.y ^ t)) | ((v.z ^ t) | (v.w ^ t)); }
__device__ __forceinline__ f32x4 untag(u32x4 v, unsigned m) { return __builtin_bit_cast(f32x4, v & ~m); }
#define CLOB() asm volatile("" ::: "memory")
#define TOUCH(x) asm volatile("" :: "v"(x) : "memory")
#define PIN(x) asm volatile("" : "+v"(x))
#define FENCE() __builtin_amdgcn_sched_barrier(0)
#define LDSW(off) (*(volatile __attribute__((address_space(3))) unsigned*)(lds + (off)))
constexpr int brev4(int t) { return ((t & 1) << 3) | ((t & 2) << 1) | ((t & 4) >> 1) | ((t & 8) >> 3); }
constexpr float LOG2E = 1.4426950408889634f;
__device__ __forceinline__ void lds_wait(unsigned char* lds, int off, unsigned want) {
  int spins = 0;
  while (LDSW(off) != want && spins < (SPIN_CAP << 4)) { ++spins; __builtin_amdgcn_s_sleep(1); }
  CLOB();
}
__device__ __forceinline__ void lds_add(unsigned char* lds, int off, unsigned v) { CLOB(); __hip_atomic_fetch_add((unsigned*)(lds + off), v, __ATOMIC_RELAXED, __HIP_MEMORY_SCOPE_WORKGROUP); CLOB(); }
template <int DUE> __device__ __forceinline__ void pace(unsigned vzero) {
  unsigned tv;
  asm volatile("ds_read_b32 %0, %1 offset:%c2\n"
               "P_%=:\n\ts_memrealtime vcc\n\ts_waitcnt lgkmcnt(0)\n\tv_readfirstlane_b32 vcc_hi, %0\n\ts_sub_i32 vcc_lo, vcc_lo, vcc_hi\n\t"
               "s_cmpk_lt_i32 vcc_lo, %c3\n\ts_cbranch_scc1 P_%="
               : "=&v"(tv) : "v"(vzero), "i"(L_TB), "i"(DUE) : "vcc", "scc", "memory");
}

enum { A_X, A_WQ, A_WG, A_WU, A_K, A_WS, A_V, A_WO, A_WD, A_OUT };
#define DESC(n) const void* p##n, unsigned n##n, unsigned f##n
__global__ void __launch_bounds__(576, 3)
fused_decode(DESC(x), DESC(wq), DESC(wg), DESC(wu), DESC(k), DESC(ws), DESC(v), DESC(wo), DESC(wd), DESC(out)) {
  __shared__ __attribute__((aligned(16))) unsigned char lds[L_END];
  const auto* ka = (const __attribute__((address_space(4))) rsrc_t*)__builtin_amdgcn_kernarg_segment_ptr();
  int tid = threadIdx.x, bid = blockIdx.x;
  rsrc_t rX, rWO, rWS; u64 t0; int wv, h, par, span; unsigned pfoff, ep, cpf; u32x4 x0, x1, p0, p1, p2, p3;
  { rsrc_t rP; u64 pc; int ps, t1, t2, t3, t4; unsigned poff;
    asm volatile(
      "s_load_dwordx4 %[rX], %[ka], 0x0\n\t"
      "s_load_dwordx4 %[rWO], %[ka], 0x70\n\t"
      "s_load_dwordx4 %[rWS], %[ka], 0x50\n\t"
      "s_memrealtime %[t0]\n\t"
      "v_readfirstlane_b32 %[wv], %[tid]\n\t"
      "s_and_b32 %[t1], %[bid], 7\n\t"
      "s_lshr_b32 %[t2], %[bid], 6\n\t"
      "s_lshl2_add_u32 %[h], %[t1], %[t2]\n\t"
      "s_lshr_b32 %[wv], %[wv], 6\n\t"
      "s_cbranch_scc1 S_%=\n\t"
      "s_setprio 3\n\t"
      "s_getpc_b64 %[pc]\n\t"
      "s_lshr_b32 %[t1], %[bid], 3\n\t"
      "v_lshl_add_u32 %[poff], %[t1], 2, %[tid]\n\t"
      "v_min_u32 %[poff], %c[cl], %[poff]\n\t"
      "v_lshlrev_b32 %[poff], 7, %[poff]\n\t"
      "s_mov_b64 exec, 15\n\t"
      "global_load_dword %[cpf], %[poff], %[pc]\n\t"
      "s_mov_b64 exec, -1\n\t"
      "v_mov_b32 %[poff], 0\n\t"
      "v_lshlrev_b32 %[pfoff], 2, %[tid]\n\t"
      "ds_write_b32 %[pfoff], %[poff] offset:%c[lf]\n\t"
      "s_waitcnt lgkmcnt(0)\n\t"
      "v_lshlrev_b32 %[poff], 5, %[tid]\n\t"
      "s_buffer_load_dword %[ep], %[rWS], 0x0\n\t"
      "buffer_load_dwordx4 %[x0], %[poff], %[rX], 0 offen\n\t"
      "buffer_load_dwordx4 %[x1], %[poff], %[rX], 0 offen offset:16\n\t"
      "s_barrier\n\t"
      "s_branch E_%=\n"
      "S_%=:\n\t"
      "s_bfe_u32 %[t3], %[bid], 0x30003\n\t"
      "s_cmp_lt_u32 %[wv], 5\n\t"
      "s_cbranch_scc1 Q_%=\n\t"
      "s_lshr_b32 %[t4], %[bid], 3\n\t"
      "s_cmpk_ge_u32 %[bid], 176\n\t"
      "s_cbranch_scc1 W_%=\n\t"
      "s_cmpk_ge_u32 %[bid], 88\n\t"
      "s_cselect_b32 %[ps], 0x30, 0x20\n\t"
      "s_cselect_b32 %[t2], 11, 0\n\t"
      "s_load_dwordx4 %[rP], %[ka], %[ps]\n\t"
      "s_sub_u32 %[t4], %[t4], %[t2]\n\t"
      "s_and_b32 %[par], %[t1], 1\n\t"
      "s_lshl_b32 %[t4], %[t4], 2\n\t"
      "s_sub_u32 %[t4], %[t4], %[par]\n\t"
      "s_lshl_b32 %[t4], %[t4], 6\n\t"
      "s_movk_i32 %[t2], 2752\n\t"
      "s_sub_u32 %[span], %[wv], 5\n\t"
      "s_branch J_%=\n"
      "W_%=:\n\t"
      "s_load_dwordx4 %[rP], %[ka], 0x80\n\t"
      "s_mul_i32 %[t1], %[t1], 43\n\t"
      "s_lshl2_add_u32 %[t4], %[t4], %[wv]\n\t"
      "s_add_u32 %[t1], %[t1], %[t4]\n\t"
      "s_sub_u32 %[t1], %[t1], 93\n\t"
      "s_lshl_b32 %[t1], %[t1], 12\n\t"
      "v_and_b32 %[poff], 63, %[tid]\n\t"
      "s_mov_b32 %[par], 0\n\t"
      "s_mov_b32 %[span], 0\n\t"
      "v_lshl_add_u32 %[poff], %[poff], 4, %[t1]\n\t"
      "s_waitcnt lgkmcnt(0)\n\t"
      "buffer_load_dwordx4 %[p0], %[poff], %[rP], 0 offen\n\t"
      "buffer_load_dwordx4 %[p1], %[poff], %[rP], 0 offen offset:1024\n\t"
      "buffer_load_dwordx4 %[p2], %[poff], %[rP], 0 offen offset:2048\n\t"
      "buffer_load_dwordx4 %[p3], %[poff], %[rP], 0 offen offset:3072\n\t"
      "s_branch B_%=\n"
      "Q_%=:\n\t"
      "s_load_dwordx4 %[rP], %[ka], 0x10\n\t"
      "s_mov_b32 %[t1], %[t3]\n\t"
      "s_and_b32 %[par], %[t3], 1\n\t"
      "s_lshl_b32 %[t4], %[h], 8\n\t"
      "s_movk_i32 %[t2], 8192\n\t"
      "s_sub_u32 %[span], %[wv], 1\n"
      "J_%=:\n\t"
      "v_lshrrev_b32 %[poff], 1, %[tid]\n\t"
      "s_lshr_b32 %[t1], %[t1], 1\n\t"
      "v_and_b32 %[pfoff], 15, %[tid]\n\t"
      "s_lshl2_add_u32 %[span], %[t1], %[span]\n\t"
      "v_lshl_add_u32 %[pfoff], %[pfoff], 4, %[t4]\n\t"
      "s_lshl_b32 %[t1], %[span], 5\n\t"
      "s_or_b32 %[t1], %[t1], %[par]\n\t"
      "v_cmp_gt_u32 vcc, %[t2], %[pfoff]\n\t"
      "v_and_or_b32 %[poff], %[poff], 24, %[t1]\n\t"
      "s_lshl_b32 %[t4], %[t2], 1\n\t"
      "v_mad_u32_u24 %[poff], %[poff], %[t2], %[pfoff]\n\t"
      "v_mov_b32 %[pfoff], 0x7ff00000\n\t"
      "s_lshl_b32 %[t1], %[t2], 2\n\t"
      "v_cndmask_b32 %[poff], %[pfoff], %[poff], vcc\n\t"
      "s_mul_i32 %[t2], %[t2], 6\n\t"
      "s_waitcnt lgkmcnt(0)\n\t"
      "buffer_load_dwordx4 %[p0], %[poff], %[rP], 0 offen nt\n\t"
      "buffer_load_dwordx4 %[p1], %[poff], %[rP], %[t4] offen nt\n\t"
      "buffer_load_dwordx4 %[p2], %[poff], %[rP], %[t1] offen nt\n\t"
      "buffer_load_dwordx4 %[p3], %[poff], %[rP], %[t2] offen nt\n"
      "B_%=:\n\t"
      "s_lshl3_add_u32 %[t3], %[h], %[t3]\n\t"
      "s_lshl_b32 %[t3], %[t3], 14\n\t"
      "s_sub_u32 %[t3], %[t3], 0x400\n\t"
      "v_lshl_add_u32 %[pfoff], %[tid], 4, %[t3]\n\t"
      "s_barrier\n"
      "E_%=:"
      : [rX] "=&s"(rX), [rP] "=&s"(rP), [rWO] "=&s"(rWO), [rWS] "=&s"(rWS), [t0] "=&s"(t0), [wv] "=&s"(wv), [h] "=&s"(h), [par] "=&s"(par), [span] "=&s"(span),
        [ps] "=&s"(ps), [poff] "=&v"(poff), [pfoff] "=&v"(pfoff), [cpf] "=&v"(cpf), [x0] "=&v"(x0), [x1] "=&v"(x1), [p0] "=&v"(p0), [p1] "=&v"(p1), [p2] "=&v"(p2), [p3] "=&v"(p3), [ep] "=&s"(ep),
        [pc] "=&s"(pc), [t1] "=&s"(t1), [t2] "=&s"(t2), [t3] "=&s"(t3), [t4] "=&s"(t4)
      : [ka] "s"(ka), [bid] "s"(bid), [tid] "v"(tid), [cl] "i"(CODE_LINES - 1), [lf] "i"(L_F)
      : "vcc", "scc", "memory");
  }
  asm("" : "+s"(rX)); asm("" : "+s"(rWO)); asm("" : "+s"(rWS)); asm("" : "+s"(wv)); asm("" : "+s"(h)); asm("" : "+s"(par)); asm("" : "+s"(span)); asm("" : "+s"(t0));
  static_assert(WS_EPOCH == 0 && 2 * FF == 2752 && OOB == 0x7ff00000u && A_WO == 7 && A_WS == 5 && A_WD == 8 && L_F + 256 <= L_END, "constants of the block above");
#define INDICES   \
  asm volatile("" : "+s"(bid), "+s"(wv)); asm volatile("" : "+v"(tid)); \
  const int lane = tid & 63, g = lane >> 4, c = lane & 15, xcd = bid & 7, j = bid >> 3; \
  const int hq = j >> 3, b = (j >> 2) & 1, s = j & 3, i16 = hq * 4 + s; \
  const int dcnt = i16 < 11 ? 12 : 8, dstart = xcd * 172 + 8 * i16 + 4 * min(i16, 11)

  if (wv == 0) {
    INDICES;
    const unsigned tstart = (unsigned)t0;
    if (lane < 9) LDSW(L_TB + lane * 4) = tstart - (1u << 30);
    FENCE();
    {
      float e[16];
      asm volatile("s_waitcnt vmcnt(0)" : "+v"(x0), "+v"(x1), "+v"(cpf) :: "memory");
      sfor<4>([&](auto ec) INL { constexpr int k = decltype(ec)::v; const unsigned u0 = x0[k], u1 = x1[k];
        e[2 * k] = bflo(u0); e[2 * k + 1] = bfhi(u0); e[8 + 2 * k] = bflo(u1); e[9 + 2 * k] = bfhi(u1); });
      float a = (((e[0] + e[1]) + (e[2] + e[3])) + ((e[4] + e[5]) + (e[6] + e[7]))) + (((e[8] + e[9]) + (e[10] + e[11])) + ((e[12] + e[13]) + (e[14] + e[15])));
      a = row_sum(a); a = sw16(a, a) * (1.f / 512.f);
      float v0 = 0.f, v1 = 0.f;
      sfor<8>([&](auto ec) INL { constexpr int k = decltype(ec)::v; const float d0 = e[k] - a, d1 = e[8 + k] - a; v0 = fmaf(d0, d0, v0); v1 = fmaf(d1, d1, v1); });
      float v = row_sum(v0 + v1); v = sw16(v, v) * (1.f / 512.f);
      const float mean = rbf(a), rden = __builtin_amdgcn_rcpf(rbf(__builtin_amdgcn_sqrtf(rbf(rbf(v) + 1e-5f))));
      u32x4 n0, n1;
      sfor<4>([&](auto ec) INL { constexpr int k = decltype(ec)::v;
        const unsigned d0 = pkbf(e[2 * k] - mean, e[2 * k + 1] - mean), d1 = pkbf(e[8 + 2 * k] - mean, e[9 + 2 * k] - mean);
        n0[k] = pkbf(bflo(d0) * rden, bfhi(d0) * rden); n1[k] = pkbf(bflo(d1) * rden, bfhi(d1) * rden); });
      *(u32x4*)(lds + L_XN + lane * 32) = n0; *(u32x4*)(lds + L_XN + lane * 32 + 16) = n1;
    }
    CLOB(); LDSW(F_XN) = 1u;
    FENCE();
    asm volatile("s_waitcnt lgkmcnt(0)" : "+s"(ep) :: "memory");
    const rsrc_t rV = ka[A_V], rOut = ka[A_OUT];
    unsigned ooff = (lane < 16 && s == 0) ? lane * 2 : OOB;
    const unsigned xr = __builtin_amdgcn_raw_buffer_load_b32(rX, ooff & ~2u, b * 1024 + h * 32, 0);
    const unsigned tl0 = __builtin_amdgcn_raw_buffer_load_b32(rV, 0, ((b * 32 + h) * 4 + s) << 18, NT), tl1 = __builtin_amdgcn_raw_buffer_load_b32(rOut, ooff & ~2u, b * 1024 + h * 32, 0);
    const unsigned tag = ((ep * 747796405u + 2891336453u) & ~0xfu) | ((ep + 1u) & 0xfu);
    LDSW(L_TAG) = tag;
    const int mat = bid >= 88;
    const unsigned dQ = WS_QX + (h * 8 + (j & 7)) * 1024, dF = WS_FX + (mat * 8 + xcd) * (2 * FROW * 4) + ((j - 11 * mat) * 128 + 32 - 32 * (xcd & 1)) * 4;
    unsigned fvo = bid < 176 ? (lane & 31) * 16 + (lane >> 5) * (FROW * 4) : OOB, qo = (lane >> 5) * 1024 + (lane & 31) * 16;
    const bool fon = 4 * (lane & 3) < dcnt;
    unsigned fo = fon ? (lane >> 2) * (2 * FROW * 4) + (lane & 3) * 16 : OOB, fmask = fon ? 0xffu : 0u;
    const unsigned fbase = WS_FX + b * (FROW * 4) + (32 + dstart) * 4;
    PIN(fvo); PIN(qo); PIN(fo); PIN(fmask);
    lds_wait(lds, F_PL, 8);
    { const unsigned char* src = lds + L_PL + lane * 16;
      const f32x4 vq = (*(const f32x4*)src + *(const f32x4*)(src + 1024)) + (*(const f32x4*)(src + 2048) + *(const f32x4*)(src + 3072));
      const f32x4 vf = (*(const f32x4*)(src + 4096) + *(const f32x4*)(src + 5120)) + (*(const f32x4*)(src + 6144) + *(const f32x4*)(src + 7168));
      __builtin_amdgcn_raw_buffer_store_b128(tagged4(vq, 3u, tag), rWS, lane * 16, dQ, SC1);
      __builtin_amdgcn_raw_buffer_store_b128(tagged4(vf, 0xffu, tag), rWS, fvo, dF, SC1);
    }
    asm volatile("s_waitcnt vmcnt(0)" ::: "memory");
    { u32x4 qp[4], fp; int spins = 0;
      _Pragma("unroll 1") do {
        CLOB();
        sfor<4>([&](auto kc) INL { constexpr int k = decltype(kc)::v; qp[k] = ld16(rWS, qo, WS_QX + h * 8192 + b * 512 + k * 2048, SC1); });
        fp = ld16(rWS, fo, fbase, SC1);
      } while (any((((mism(qp[0], tag) | mism(qp[1], tag) | mism(qp[2], tag) | mism(qp[3], tag)) & 3u) | (mism(fp, tag) & fmask)) != 0) && ++spins < SPIN_CAP);
      const f32x4 qs = (untag(qp[0], 3u) + untag(qp[1], 3u)) + (untag(qp[2], 3u) + untag(qp[3], 3u));
      *(u32x2*)(lds + L_Q + (lane & 31) * 8) = u32x2{pkbf(sw32(qs.x, qs.x), sw32(qs.y, qs.y)), pkbf(sw32(qs.z, qs.z), sw32(qs.w, qs.w))};
      const f32x4 f = untag(fp, 0xffu);
      float fa = sw16(f.x, f.y), fb = sw16(f.z, f.w);
      fa += dppf<ROR8>(fa); fb += dppf<ROR8>(fb); fa += dppf<ROR4>(fa); fb += dppf<ROR4>(fb);
      const auto gu = __builtin_amdgcn_permlane32_swap(__float_as_uint(fa), __float_as_uint(fb), false, false);
      const float gv = rbf(__uint_as_float(gu[0])), uv = rbf(__uint_as_float(gu[1]));
      const float z = 0.7978845608028654f * (gv + 0.044715f * gv * gv * gv);
      const float ge = rbf(gv * __builtin_amdgcn_rcpf(1.f + __builtin_amdgcn_exp2f(-2.f * LOG2E * z)));
      if (c < 4) *(u16*)(lds + L_H + (4 * c + g) * 2) = (u16)pkbf(ge * uv, 0.f);
      CLOB(); LDSW(F_Q) = 1u;
    }
    TOUCH(tl0); TOUCH(tl1); TOUCH(xr);
    { lds_wait(lds, F_KM, 8);
      const unsigned tK = tstart + ((LDSW(L_T2) - 8u * tstart) >> 3);
      __builtin_amdgcn_raw_buffer_store_b64(u32x2{tK, tag}, rWS, (lane == 0 && s == hq && b == (hq & 1)) ? 0u : OOB, WS_TM + h * 8, SC1);
      for (int k = 0; k < TDELAY; ++k) __builtin_amdgcn_s_sleep(16);
      const int dn = (int)(now() - tK);
      const u32x2 ta = __builtin_amdgcn_raw_buffer_load_b64(rWS, (lane & 31) * 8, WS_TM, SC1);
      const float mt = wave_sum((float)(ta.y == tag ? (int)(ta.x - tK) : dn)) * (1.f / 64.f);
      if (lane < 9) LDSW(L_TB + lane * 4) = tK + (int)fminf(mt, 400.f) + (lane ? ((2 * lane - 9) * STAGW) >> 5 : 0);
    }
    { const unsigned mb = WS_ML + (b * 32 + h) * 128;
      lds_wait(lds, F_ML, 8);
      const f32x2 ml = *(const f32x2*)(lds + L_ML + (lane & 7) * 8);
      float m = fmaxf(ml.x, dppf<XOR1>(ml.x)); m = fmaxf(m, dppf<XOR2>(m)); m = fmaxf(m, dppf<HMIR>(m));
      float l = ml.y * __builtin_amdgcn_exp2f((ml.x - m) * LOG2E); l += dppf<XOR1>(l); l += dppf<XOR2>(l); l += dppf<HMIR>(l);
      __builtin_amdgcn_raw_buffer_store_b128(u32x4{__float_as_uint(m), tag, __float_as_uint(l), tag}, rWS, lane == 0 ? 0u : OOB, mb + s * 16, SC1);
      __builtin_amdgcn_s_sleep(127);
      u32x4 mp; int spins = 0;
      _Pragma("unroll 1") do {
        __builtin_amdgcn_s_sleep(8); CLOB();
        mp = ld16(rWS, (lane & 3) * 16, mb, SC1);
      } while (any(mp.y != tag || mp.w != tag) && ++spins < SPIN_CAP);
      const float mj = __uint_as_float(mp.x), lj = __uint_as_float(mp.z);
      float M = fmaxf(mj, dppf<XOR1>(mj)); M = fmaxf(M, dppf<XOR2>(M));
      float L = lj * __builtin_amdgcn_exp2f((mj - M) * LOG2E); L += dppf<XOR1>(L); L += dppf<XOR2>(L);
      *(f32x2*)(lds + L_MR) = f32x2{M, __builtin_amdgcn_rcpf(L)};
      CLOB(); LDSW(F_MR) = 1u;
    }
    unsigned rrd = L_RL + c * 32; float xf = (lane & 1) ? bfhi(xr) : bflo(xr);
    PIN(rrd); PIN(xf); PIN(ooff);
    wg_barrier();
    if (s != 0) return;
    wg_barrier();
    const f32x4 r4 = *(const f32x4*)(lds + rrd) + *(const f32x4*)(lds + rrd + 16);
    __builtin_amdgcn_raw_buffer_store_b16((u16)pkbf(xf + ((r4.x + r4.y) + (r4.z + r4.w)), 0.f), rOut, ooff, b * 1024 + h * 32, 0);
    if (bid == 0) __builtin_amdgcn_raw_buffer_store_b32(tag, rWS, lane == 0 ? 0u : OOB, WS_EPOCH, 0);
    return;
  }

  u32x4 PF[2], KV[64];
  PF[0] = ld16(rWO, pfoff, 0, 0); PF[1] = ld16(rWO, pfoff, 8192, 0);
  CLOB(); FENCE();
  const rsrc_t rK = ka[A_K], rV = ka[A_V], rWD = ka[A_WD];
  INDICES;
  const int w = wv - 1, rot = (bid << 12) & 0x38000;
  unsigned kvoff = (((b * 32 + h) * 4 + s) << 18) + (tid - 64) * 16; PIN(kvoff);
  u32x4 sel, q;
  u32x2 WO[32], WD[4];
  u32x8 BO[8];
  unsigned wooff = 0, wdoff = 0, owr = 0, ord = 0, yst = 0, yld = 0, rwr = 0, ytag = 0, vzero = 0, vw4 = wv * 4;
  int sidx = 0;
  f32x4 oacc = {0.f, 0.f, 0.f, 0.f}, yacc[4];
  float t1 = 0.f, t2 = 0.f, t3 = 0.f, t4 = 0.f, sc[2] = {0.f, 0.f}, mw = 0.f, cw = 0.f;
  unsigned pp[2] = {0u, 0u};
  auto mkA = [&](unsigned src) INL -> u32x4 { u32x4 a; a.x = __builtin_amdgcn_perm(src, src, sel.x); a.y = __builtin_amdgcn_perm(src, src, sel.y);
                                              a.z = __builtin_amdgcn_perm(src, src, sel.z); a.w = __builtin_amdgcn_perm(src, src, sel.w); return a; };
  auto tile_off = [&](int t) INL { int r = rot; asm volatile("" : "+s"(r)); return (r + t * 8192) & 0x3ffff; };

  auto consumeK = [&](auto tc) INL {
    constexpr int t = decltype(tc)::v;
    const u32x4 k = KV[t];
    float d = dot2(k.x, q.x, 0.f); d = dot2(k.y, q.y, d); d = dot2(k.z, q.z, d); d = dot2(k.w, q.w, d);
    if constexpr ((t & 1) == 0) { t1 = d; PIN(t1); }
    else {
      { const float keep = (c & 8) ? d : t1, send = (c & 8) ? t1 : d; d = keep + dppf<ROR8>(send); }
      if constexpr ((t & 2) == 0) { t2 = d; PIN(t2); }
      else {
        { const float keep = (c & 4) ? d : t2, send = (c & 4) ? t2 : d; d = keep + dppf<HMIR>(send); }
        if constexpr ((t & 4) == 0) { t3 = d; PIN(t3); }
        else {
          { const float keep = (c & 2) ? d : t3, send = (c & 2) ? t3 : d; d = keep + dppf<XOR2>(send); }
          if constexpr ((t & 8) == 0) { t4 = d; PIN(t4); }
          else { const float keep = (c & 1) ? d : t4, send = (c & 1) ? t4 : d; sc[t >> 4] = keep + dppf<XOR1>(send); PIN(sc[t >> 4]); }
        }
      }
    }
    if constexpr (t == 31) {
      const float s0 = rbf(rbf(sc[0]) * 0.08838834764831845f), s1 = rbf(rbf(sc[1]) * 0.08838834764831845f);
      mw = wave_max(fmaxf(s0, s1));
      const float p0 = __builtin_amdgcn_exp2f((s0 - mw) * LOG2E), p1 = __builtin_amdgcn_exp2f((s1 - mw) * LOG2E);
      const float lw = wave_sum(p0 + p1);
      pp[0] = pkbf(p0, p0 - rbf(p0)); pp[1] = pkbf(p1, p1 - rbf(p1)); PIN(pp[0]); PIN(pp[1]);
      if (lane == 0) { *(f32x2*)(lds + L_ML + w * 8) = f32x2{mw, lw}; lds_add(lds, F_ML, 1u); }
    }
  };

  constexpr Sched S{};
  sfor<S.n>([&](auto nc) INL {
    constexpr Act A = S.a[decltype(nc)::v];
    constexpr int i = A.i;
    if constexpr (A.issue) {
      if constexpr (A.t == T_K || A.t == T_V) {
        constexpr int n = i + (A.t == T_V ? 32 : 0);
        if constexpr (n >= APACE0) { pace<(AEND - (63 - n) * ATAU) / 16>(vw4); FENCE(); }
        KV[n] = ld16(A.t == T_V ? rV : rK, kvoff, tile_off(i), NT);
      }
      else if constexpr (A.t == T_WO) WO[i] = __builtin_amdgcn_raw_buffer_load_b64(rWO, wooff, ((i >> 3) * 32 + (i & 7)) * 1024, NT);
      else if constexpr (A.t == T_WD) WD[i] = __builtin_amdgcn_raw_buffer_load_b64(rWD, wdoff, i * 1024, NT);
    } else {
      if constexpr (A.t == T_P) {
        if constexpr (i == 0) {
          const unsigned sb = (c & 8) ? 0x0302u : 0x0100u, one = (c & 1) ? ((sb << 16) | 0x0c0cu) : (0x0c0c0000u | sb); const int v = (c & 7) >> 1;
          sel.x = v == 0 ? one : 0x0c0c0c0cu; sel.y = v == 1 ? one : 0x0c0c0c0cu; sel.z = v == 2 ? one : 0x0c0c0c0cu; sel.w = v == 3 ? one : 0x0c0c0c0cu;
          wooff = h * 131072 + g * 8192 + w * 128 + c * 8;
          wdoff = (4 * g < dcnt) ? (unsigned)(dstart * 1024 + g * 4096 + w * 128 + c * 8) : OOB;
          owr = L_OL + (w + (lane >= 32 ? 8 : 0)) * OL_STR + c * 8 + (g & 1) * 4;
          ord = L_OL + c * OL_STR + g * 16;
          sidx = (c & 8) ? 0xeeee : 0x4444;
          yst = g ? OOB : WS_Y + (b * 128 + h * 4 + s) * 2048 + w * 256 + c * 16;
          yld = WS_Y + b * 128 * 2048 + h * 64 + ((tid - 64) >> 2) * 2048 + (lane & 3) * 16;
          rwr = c < 4 ? L_RL + (4 * c + g) * 32 + w * 4 : L_PL + lane * 4;
          PIN(sel); PIN(wooff); PIN(wdoff); PIN(owr); PIN(ord); PIN(sidx); PIN(yst); PIN(yld); PIN(rwr); PIN(vzero); PIN(vw4);
        }
        if constexpr (i < 3) asm volatile("s_waitcnt vmcnt(5)" ::: "memory");
        else asm volatile("s_waitcnt vmcnt(5)" : "+v"(p0), "+v"(p1), "+v"(p2), "+v"(p3) :: "memory");
        if constexpr (i == 3) {
          lds_wait(lds, F_XN, 1u);
          const u32x4 xa = *(const u32x4*)(lds + L_XN + (c >> 3) * 1024 + g * 16 + span * 64);
          f32x4 pacc = {0.f, 0.f, 0.f, 0.f};
          const u32x4 P[4] = {p0, p1, p2, p3};
          sfor<4>([&](auto ec) INL { constexpr int e = decltype(ec)::v; const unsigned u = xa[e];
            pacc = mfma32(mkA(__builtin_amdgcn_perm(u, u, par ? 0x03020302u : 0x01000100u)), P[e], pacc); });
          *(f32x4*)(lds + L_PL + w * 1024 + (g >> 1) * 512 + c * 32 + (g & 1) * 16) = pacc;
          if (lane == 0) lds_add(lds, F_PL, 1u);
        }
      } else if constexpr (A.t == T_PF) { TOUCH(PF[i]);
      } else if constexpr (A.t == T_K) {
        TOUCH(KV[i]);
        if constexpr (i == KM) { const unsigned t = now(); if (lane == 0) { lds_add(lds, L_T2, t); lds_add(lds, F_KM, 1u); } }
        if constexpr (i == QPOS) {
          lds_wait(lds, F_Q, 1u);
          q = *(const u32x4*)(lds + L_Q + c * 16);
          ytag = LDSW(L_TAG) & 0xfu;
          PIN(ytag);
        }
      } else if constexpr (A.t == T_WD) {
        TOUCH(WD[i]);
        if constexpr (i == 3) {
          u32x2 hA = *(const u32x2*)(lds + L_H + g * 8);
          if (c != 0) hA = u32x2{0u, 0u};
          sfor<4>([&](auto tc) INL { constexpr int t = decltype(tc)::v; constexpr unsigned ps = (t & 1) ? 0x07060302u : 0x05040100u;
            const unsigned w0 = WD[0][t >> 1], w1 = WD[1][t >> 1], w2 = WD[2][t >> 1], w3 = WD[3][t >> 1];
            const u32x2 bb = {__builtin_amdgcn_perm(w1, w0, ps), __builtin_amdgcn_perm(w3, w2, ps)};
            yacc[t] = mfma16(hA, bb, f32x4{0.f, 0.f, 0.f, 0.f}); PIN(yacc[t]); });
        }
      } else if constexpr (A.t == T_WO) {
        TOUCH(WO[i]);
        if constexpr ((i & 7) == 7) {
          constexpr int u = i >> 3;
          sfor<4>([&](auto tc) INL { constexpr int t = decltype(tc)::v; constexpr unsigned ps = (t & 1) ? 0x07060302u : 0x05040100u;
            sfor<4>([&](auto vc) INL { constexpr int v = decltype(vc)::v;
              const unsigned lo = WO[u * 8 + 2 * v][t >> 1], hi = WO[u * 8 + 2 * v + 1][t >> 1];
              BO[(u >> 1) * 4 + t][(u & 1) * 4 + v] = __builtin_amdgcn_perm(hi, lo, ps); });
            PIN(BO[(u >> 1) * 4 + t]); });
        }
      } else if constexpr (A.t == T_V) {
        oacc = mfma32(mkA(dppu<BCAST + brev4(i & 15)>(pp[i >> 4])), KV[32 + i], oacc); PIN(oacc);
        if constexpr (i == 27) {
          lds_wait(lds, F_MR, 1u);
          const f32x2 mr = *(const f32x2*)(lds + L_MR);
          cw = __builtin_amdgcn_exp2f((mw - mr.x) * LOG2E) * mr.y;
          PIN(cw);
        }
      }
      sfor<A.k1 - A.k0>([&](auto kc) INL { FENCE(); consumeK(IC<A.k0 + decltype(kc)::v>{}); });
    }
    FENCE();
  });

  *(unsigned*)(lds + owr) = pkbf(sw32(oacc.x, oacc.z) * cw, sw32(oacc.y, oacc.w) * cw);
  wg_barrier();
  { const u32x4 a0 = *(const u32x4*)(lds + ord), a1 = *(const u32x4*)(lds + ord + 64);
    FENCE();
    auto sum4 = [&](f32x4 u) INL { const f32x2 p = f32x2{u.x, u.y} + f32x2{u.z, u.w}; return p.x + p.y; };
    auto fin = [&](float y) INL { y = sw16(y, y); const unsigned t = ((__float_as_uint(y) + 8u) & ~0xfu) | ytag; return __builtin_amdgcn_permlane32_swap(t, t, false, false); };
    yacc[0] = smfma64(a0, BO[0], yacc[0], sidx); yacc[2] = smfma64(a0, BO[2], yacc[2], sidx); FENCE();
    yacc[0] = smfma64(a1, BO[4], yacc[0], sidx); yacc[2] = smfma64(a1, BO[6], yacc[2], sidx); FENCE();
    yacc[1] = smfma64(a0, BO[1], yacc[1], sidx); FENCE();
    float z0 = sum4(yacc[0]); PIN(z0); FENCE();
    yacc[3] = smfma64(a0, BO[3], yacc[3], sidx); FENCE();
    float z2 = sum4(yacc[2]); PIN(z2); FENCE();
    yacc[1] = smfma64(a1, BO[5], yacc[1], sidx); FENCE();
    float ya = sw32(z0, z2); PIN(ya); FENCE();
    yacc[3] = smfma64(a1, BO[7], yacc[3], sidx); FENCE();
    const auto sa = fin(ya);
    const auto sb = fin(sw32(sum4(yacc[1]), sum4(yacc[3])));
    __builtin_amdgcn_raw_buffer_store_b128(u32x4{sa[0], sb[0], sa[1], sb[1]}, rWS, yst, 0, SC1);
  }
  if (s != 0) return;
  { u32x4 y; int spins = SPIN_CAP;
    pace<AEND / 16 + YPOLL>(vzero);
    unsigned t0_, t1_;
    asm volatile("s_waitcnt vmcnt(0)\n"
                 "L_%=:\n\tbuffer_load_dwordx4 v[2:5], %[off], %[rs], 0 offen sc1\n\ts_waitcnt vmcnt(0)\n\t"
                 "v_bitop3_b32 %[t0], v2, v3, %[tag] bitop3:0x7e\n\tv_bitop3_b32 %[t1], v4, v5, %[tag] bitop3:0x7e\n\t"
                 "v_bitop3_b32 %[t0], %[t0], %[t1], 15 bitop3:0xa8\n\tv_cmp_ne_u32 vcc, 0, %[t0]\n\ts_cbranch_vccz D_%=\n\t"
                 "s_sub_u32 %[sp], %[sp], 1\n\ts_cbranch_scc0 L_%=\n"
                 "D_%=:"
                 : "=&{v[2:5]}"(y), [t0] "=&v"(t0_), [t1] "=&v"(t1_), [sp] "+s"(spins) : [off] "v"(yld), [rs] "s"(rWS), [tag] "v"(ytag) : "vcc", "scc", "memory");
    float v = tr4(untag(y, 0xfu)); v += dppf<ROR8>(v); v += dppf<ROR4>(v);
    *(float*)(lds + rwr) = v;
    wg_barrier();
  }
}
}

extern "C" void kernel_launch(void* const* d_in, const int* in_sizes, int n_in, void* d_out, int out_size, void* d_ws, size_t ws_size, hipStream_t stream) {
  const unsigned F = 0x00020000u;
  fused_decode<<<256, 576, 0, stream>>>(d_in[0], 2048u, F, d_in[3], 512u * 8192u, F, d_in[6], 512u * 2u * FF, F, d_in[7], 512u * 2u * FF, F, d_in[1], 64u << 20, F,
                                        d_ws, WS_END, F, d_in[2], 64u << 20, F, d_in[4], 4096u * 1024u, F, d_in[8], FF * 1024u, F, d_out, 2048u, F);
}
```

```cpp
#include <hip/hip_runtime.h>
#include <stdint.h>
#include <utility>

#define CODE_LINES 114
#define WLIM 40
#define QPOS 14
#define WO0 3
#define WO1 59
#define WD_POS 22
#define KM 16
#define TDELAY 5
#define APACE0 29
#define ATAU 450
#define AEND 19750
#define STAGW 56
#define YPOLL 125

namespace {
typedef unsigned u32x8 __attribute__((ext_vector_type(8)));
typedef unsigned u32x4 __attribute__((ext_vector_type(4)));
typedef unsigned u32x2 __attribute__((ext_vector_type(2)));
typedef float f32x4 __attribute__((ext_vector_type(4)));
typedef float f32x2 __attribute__((ext_vector_type(2)));
typedef __bf16 bf16x16 __attribute__((ext_vector_type(16)));
typedef __bf16 bf16x8 __attribute__((ext_vector_type(8)));
typedef __bf16 bf16x2 __attribute__((ext_vector_type(2)));
typedef short s16x4 __attribute__((ext_vector_type(4)));
typedef unsigned short u16;
typedef unsigned long long u64;
typedef __amdgpu_buffer_rsrc_t rsrc_t;

#define INL __attribute__((always_inline))
template <int I> struct IC { static constexpr int v = I; };
template <class F, int... Is> __device__ __forceinline__ void sfor_impl(F&& f, std::integer_sequence<int, Is...>) { (f(IC<Is>{}), ...); }
template <int N, class F> __device__ __forceinline__ void sfor(F&& f) { sfor_impl(f, std::make_integer_sequence<int, N>{}); }

constexpr int NT = 2, SC1 = 16;
constexpr int FF = 1376, FROW = 1440;
constexpr unsigned WS_EPOCH = 0;
constexpr unsigned WS_ML = 4096;
constexpr unsigned WS_TM = WS_ML + 64 * 128;
constexpr unsigned WS_QX = WS_TM + 2048;
constexpr unsigned WS_FX = WS_QX + 32 * 8 * 1024;
constexpr unsigned WS_Y = 1u << 20;
static_assert(WS_FX + 2 * 2 * 8 * FROW * 4 <= WS_Y, "ws map");
constexpr unsigned WS_END = WS_Y + 2 * 128 * 2048;
constexpr unsigned OOB = 0x7ff00000u;
constexpr int SPIN_CAP = 1 << 18;
constexpr int L_PL = 0, L_XN = 8192, L_Q = L_XN + 2048, L_H = L_Q + 256, L_ML = L_H + 32, L_MR = L_ML + 64, L_F = L_MR + 16,
              F_PL = L_F, F_Q = L_F + 4, F_ML = L_F + 8, F_MR = L_F + 12, F_KM = L_F + 16, F_XN = L_F + 20, L_T2 = L_F + 28,
              L_TAG = L_F + 32, L_TB = L_F + 44, L_RL = L_F + 80, L_OL = L_RL + 512, OL_STR = 144, L_END = L_OL + 16 * OL_STR;

enum { T_P, T_PF, T_K, T_V, T_WO, T_WD };
struct Item { int t, i; };
struct Act { int issue; int t, i; int k0, k1; };
constexpr int weight(int t) { return (t == T_WO || t == T_WD) ? 4 : 8; }
struct Sched {
  Act a[240]; int n;
  constexpr Sched() : a{}, n(0) {
    Item it[120] = {}; int ni = 0;
    for (int i = 0; i < 4; ++i) it[ni++] = Item{T_P, i};
    for (int i = 0; i < 2; ++i) it[ni++] = Item{T_PF, i};
    int wo = 0;
    for (int k = 0; k < 64; ++k) {
      it[ni++] = k < 32 ? Item{T_K, k} : Item{T_V, k - 32};
      if (k >= WD_POS && k < WD_POS + 4) it[ni++] = Item{T_WD, k - WD_POS};
      while (wo < 32 && k >= WO0 && wo * (WO1 - WO0) < (k - WO0 + 1) * 32) it[ni++] = Item{T_WO, wo++};
    }
    int head = 0, fl = 0, kdone = 0, karr = 0;
    for (int j = 0; j <= ni; ++j) {
      while (head < j && (j == ni || fl + weight(it[j].t) > (head < 4 ? 48 : WLIM))) {
        Item r = it[head++]; fl -= weight(r.t);
        if (r.t == T_K) karr = r.i + 1;
        int k0 = kdone;
        if (karr > QPOS) kdone = (karr == 32 || kdone + 5 >= karr) ? karr : kdone + 5;
        a[n++] = Act{0, r.t, r.i, k0, kdone};
      }
      if (j < ni) { a[n++] = Act{1, it[j].t, it[j].i, 0, 0}; fl += weight(it[j].t); }
    }
  }
};

static_assert(WD_POS > QPOS, "h is written with q");

__device__ __forceinline__ float bflo(unsigned u) { return __uint_as_float(u << 16); }
__device__ __forceinline__ float bfhi(unsigned u) { return __uint_as_float(u & 0xffff0000u); }
__device__ __forceinline__ unsigned pkbf(float lo, float hi) { f32x2 f = {lo, hi}; bf16x2 h = __builtin_convertvector(f, bf16x2); return __builtin_bit_cast(unsigned, h); }
__device__ __forceinline__ float rbf(float f) { return bflo(pkbf(f, f)); }
__device__ __forceinline__ float dot2(unsigned a, unsigned b, float c) { return __builtin_amdgcn_fdot2_f32_bf16(__builtin_bit_cast(bf16x2, a), __builtin_bit_cast(bf16x2, b), c, false); }
template <int CTRL> __device__ __forceinline__ unsigned dppu(unsigned x) { return __builtin_amdgcn_update_dpp(0u, x, CTRL, 0xf, 0xf, true); }
template <int CTRL> __device__ __forceinline__ float dppf(float x) { return __uint_as_float(dppu<CTRL>(__float_as_uint(x))); }
constexpr int XOR1 = 0xB1, XOR2 = 0x4E, HMIR = 0x141, ROR8 = 0x128, ROR4 = 0x124, BCAST = 0x150;
__device__ __forceinline__ float row_sum(float x) { x += dppf<XOR1>(x); x += dppf<XOR2>(x); x += dppf<HMIR>(x); x += dppf<ROR8>(x); return x; }
__device__ __forceinline__ float row_max(float x) { x = fmaxf(x, dppf<XOR1>(x)); x = fmaxf(x, dppf<XOR2>(x)); x = fmaxf(x, dppf<HMIR>(x)); x = fmaxf(x, dppf<ROR8>(x)); return x; }
__device__ __forceinline__ float sw16(float a, float b) { auto s = __builtin_amdgcn_permlane16_swap(__float_as_uint(a), __float_as_uint(b), false, false); return __uint_as_float(s[0]) + __uint_as_float(s[1]); }
__device__ __forceinline__ float sw32(float a, float b) { auto s = __builtin_amdgcn_permlane32_swap(__float_as_uint(a), __float_as_uint(b), false, false); return __uint_as_float(s[0]) + __uint_as_float(s[1]); }
__device__ __forceinline__ float x16_max(float x) { auto s = __builtin_amdgcn_permlane16_swap(__float_as_uint(x), __float_as_uint(x), false, false); return fmaxf(__uint_as_float(s[0]), __uint_as_float(s[1])); }
__device__ __forceinline__ float x32_max(float x) { auto s = __builtin_amdgcn_permlane32_swap(__float_as_uint(x), __float_as_uint(x), false, false); return fmaxf(__uint_as_float(s[0]), __uint_as_float(s[1])); }
__device__ __forceinline__ float wave_sum(float x) { x = row_sum(x); x = sw16(x, x); return sw32(x, x); }
__device__ __forceinline__ float wave_max(float x) { return x32_max(x16_max(row_max(x))); }
__device__ __forceinline__ float tr4(f32x4 z) { return sw16(sw32(z.x, z.z), sw32(z.y, z.w)); }
__device__ __forceinline__ f32x4 mfma32(u32x4 a, u32x4 b, f32x4 c) { return __builtin_amdgcn_mfma_f32_16x16x32_bf16(__builtin_bit_cast(bf16x8, a), __builtin_bit_cast(bf16x8, b), c, 0, 0, 0); }
__device__ __forceinline__ f32x4 mfma16(u32x2 a, u32x2 b, f32x4 c) { return __builtin_amdgcn_mfma_f32_16x16x16bf16_1k(__builtin_bit_cast(s16x4, a), __builtin_bit_cast(s16x4, b), c, 0, 0, 0); }
__device__ __forceinline__ f32x4 smfma64(u32x4 a, u32x8 b, f32x4 c, int idx) { return __builtin_amdgcn_smfmac_f32_16x16x64_bf16(__builtin_bit_cast(bf16x8, a), __builtin_bit_cast(bf16x16, b), c, idx, 0, 0); }
__device__ __forceinline__ u32x4 ld16(rsrc_t r, unsigned v, unsigned s, int aux) { return aux == NT ? __builtin_amdgcn_raw_buffer_load_b128(r, v, s, NT) : aux == SC1 ? __builtin_amdgcn_raw_buffer_load_b128(r, v, s, SC1) : __builtin_amdgcn_raw_buffer_load_b128(r, v, s, 0); }
__device__ __forceinline__ void wg_barrier() { asm volatile("s_waitcnt lgkmcnt(0)\n\ts_barrier" ::: "memory"); }
__device__ __forceinline__ bool any(bool p) { return __builtin_amdgcn_ballot_w64(p) != 0; }
__device__ __forceinline__ unsigned now() { return (unsigned)__builtin_amdgcn_s_memrealtime(); }
__device__ __forceinline__ unsigned tagged(float v, unsigned m, unsigned t) { return ((__float_as_uint(v) + ((m + 1) >> 1)) & ~m) | (t & m); }
__device__ __forceinline__ u32x4 tagged4(f32x4 v, unsigned m, unsigned t) { return u32x4{tagged(v.x, m, t), tagged(v.y, m, t), tagged(v.z, m, t), tagged(v.w, m, t)}; }
__device__ __forceinline__ unsigned mism(u32x4 v, unsigned t) { return ((v.x ^ t) | (v.y ^ t)) | ((v.z ^ t) | (v.w ^ t)); }
__device__ __forceinline__ f32x4 untag(u32x4 v, unsigned m) { return __builtin_bit_cast(f32x4, v & ~m); }
#define CLOB() asm volatile("" ::: "memory")
#define TOUCH(x) asm volatile("" :: "v"(x) : "memory")
#define PIN(x) asm volatile("" : "+v"(x))
#define FENCE() __builtin_amdgcn_sched_barrier(0)
#define LDSW(off) (*(volatile __attribute__((address_space(3))) unsigned*)(lds + (off)))
constexpr int brev4(int t) { return ((t & 1) << 3) | ((t & 2) << 1) | ((t & 4) >> 1) | ((t & 8) >> 3); }
constexpr float LOG2E = 1.4426950408889634f;
__device__ __forceinline__ void lds_wait(unsigned char* lds, int off, unsigned want) {
  int spins = 0;
  while (LDSW(off) != want && spins < (SPIN_CAP << 4)) { ++spins; __builtin_amdgcn_s_sleep(1); }
  CLOB();
}
__device__ __forceinline__ void lds_add(unsigned char* lds, int off, unsigned v) { CLOB(); __hip_atomic_fetch_add((unsigned*)(lds + off), v, __ATOMIC_RELAXED, __HIP_MEMORY_SCOPE_WORKGROUP); CLOB(); }
template <int DUE> __device__ __forceinline__ void pace(unsigned vzero) {
  unsigned tv;
  asm volatile("ds_read_b32 %0, %1 offset:%c2\n"
               "P_%=:\n\ts_memrealtime vcc\n\ts_waitcnt lgkmcnt(0)\n\tv_readfirstlane_b32 vcc_hi, %0\n\ts_sub_i32 vcc_lo, vcc_lo, vcc_hi\n\t"
               "s_cmpk_lt_i32 vcc_lo, %c3\n\ts_cbranch_scc1 P_%="
               : "=&v"(tv) : "v"(vzero), "i"(L_TB), "i"(DUE) : "vcc", "scc", "memory");
}

enum { A_X, A_WQ, A_WG, A_WU, A_K, A_WS, A_V, A_WO, A_WD, A_OUT };
#define DESC(n) const void* p##n, unsigned n##n, unsigned f##n
__global__ void __launch_bounds__(576, 3)
fused_decode(DESC(x), DESC(wq), DESC(wg), DESC(wu), DESC(k), DESC(ws), DESC(v), DESC(wo), DESC(wd), DESC(out)) {
  __shared__ __attribute__((aligned(16))) unsigned char lds[L_END];
  const auto* ka = (const __attribute__((address_space(4))) rsrc_t*)__builtin_amdgcn_kernarg_segment_ptr();
  int tid = threadIdx.x, bid = blockIdx.x;
  rsrc_t rX, rWO, rWS; u64 t0; int wv, h, par, span; unsigned pfoff, ep, cpf; u32x4 x0, x1, p0, p1, p2, p3;
  { rsrc_t rP; u64 pc; int ps, t1, t2, t3, t4; unsigned poff;
    asm volatile(
      "s_load_dwordx4 %[rX], %[ka], 0x0\n\t"
      "s_load_dwordx4 %[rWO], %[ka], 0x70\n\t"
      "s_load_dwordx4 %[rWS], %[ka], 0x50\n\t"
      "s_memrealtime %[t0]\n\t"
      "v_readfirstlane_b32 %[wv], %[tid]\n\t"
      "s_and_b32 %[t1], %[bid], 7\n\t"
      "s_lshr_b32 %[t2], %[bid], 6\n\t"
      "s_lshl2_add_u32 %[h], %[t1], %[t2]\n\t"
      "s_lshr_b32 %[wv], %[wv], 6\n\t"
      "s_cbranch_scc1 S_%=\n\t"
      "s_setprio 3\n\t"
      "s_getpc_b64 %[pc]\n\t"
      "s_lshr_b32 %[t1], %[bid], 3\n\t"
      "v_lshl_add_u32 %[poff], %[t1], 2, %[tid]\n\t"
      "v_min_u32 %[poff], %c[cl], %[poff]\n\t"
      "v_lshlrev_b32 %[poff], 7, %[poff]\n\t"
      "s_mov_b64 exec, 15\n\t"
      "global_load_dword %[cpf], %[poff], %[pc]\n\t"
      "s_mov_b64 exec, -1\n\t"
      "v_mov_b32 %[poff], 0\n\t"
      "v_lshlrev_b32 %[pfoff], 2, %[tid]\n\t"
      "ds_write_b32 %[pfoff], %[poff] offset:%c[lf]\n\t"
      "s_waitcnt lgkmcnt(0)\n\t"
      "v_lshlrev_b32 %[poff], 5, %[tid]\n\t"
      "s_buffer_load_dword %[ep], %[rWS], 0x0\n\t"
      "buffer_load_dwordx4 %[x0], %[poff], %[rX], 0 offen\n\t"
      "buffer_load_dwordx4 %[x1], %[poff], %[rX], 0 offen offset:16\n\t"
      "s_barrier\n\t"
      "s_branch E_%=\n"
      "S_%=:\n\t"
      "s_bfe_u32 %[t3], %[bid], 0x30003\n\t"
      "s_cmp_lt_u32 %[wv], 5\n\t"
      "s_cbranch_scc1 Q_%=\n\t"
      "s_lshr_b32 %[t4], %[bid], 3\n\t"
      "s_cmpk_ge_u32 %[bid], 176\n\t"
      "s_cbranch_scc1 W_%=\n\t"
      "s_cmpk_ge_u32 %[bid], 88\n\t"
      "s_cselect_b32 %[ps], 0x30, 0x20\n\t"
      "s_cselect_b32 %[t2], 11, 0\n\t"
      "s_load_dwordx4 %[rP], %[ka], %[ps]\n\t"
      "s_sub_u32 %[t4], %[t4], %[t2]\n\t"
      "s_and_b32 %[par], %[t1], 1\n\t"
      "s_lshl_b32 %[t4], %[t4], 2\n\t"
      "s_sub_u32 %[t4], %[t4], %[par]\n\t"
      "s_lshl_b32 %[t4], %[t4], 6\n\t"
      "s_movk_i32 %[t2], 2752\n\t"
      "s_sub_u32 %[span], %[wv], 5\n\t"
      "s_branch J_%=\n"
      "W_%=:\n\t"
      "s_load_dwordx4 %[rP], %[ka], 0x80\n\t"
      "s_mul_i32 %[t1], %[t1], 43\n\t"
      "s_lshl2_add_u32 %[t4], %[t4], %[wv]\n\t"
      "s_add_u32 %[t1], %[t1], %[t4]\n\t"
      "s_sub_u32 %[t1], %[t1], 93\n\t"
      "s_lshl_b32 %[t1], %[t1], 12\n\t"
      "v_and_b32 %[poff], 63, %[tid]\n\t"
      "s_mov_b32 %[par], 0\n\t"
      "s_mov_b32 %[span], 0\n\t"
      "v_lshl_add_u32 %[poff], %[poff], 4, %[t1]\n\t"
      "s_waitcnt lgkmcnt(0)\n\t"
      "buffer_load_dwordx4 %[p0], %[poff], %[rP], 0 offen\n\t"
      "buffer_load_dwordx4 %[p1], %[poff], %[rP], 0 offen offset:1024\n\t"
      "buffer_load_dwordx4 %[p2], %[poff], %[rP], 0 offen offset:2048\n\t"
      "buffer_load_dwordx4 %[p3], %[poff], %[rP], 0 offen offset:3072\n\t"
      "s_branch B_%=\n"
      "Q_%=:\n\t"
      "s_load_dwordx4 %[rP], %[ka], 0x10\n\t"
      "s_mov_b32 %[t1], %[t3]\n\t"
      "s_and_b32 %[par], %[t3], 1\n\t"
      "s_lshl_b32 %[t4], %[h], 8\n\t"
      "s_movk_i32 %[t2], 8192\n\t"
      "s_sub_u32 %[span], %[wv], 1\n"
      "J_%=:\n\t"
      "v_lshrrev_b32 %[poff], 1, %[tid]\n\t"
      "s_lshr_b32 %[t1], %[t1], 1\n\t"
      "v_and_b32 %[pfoff], 15, %[tid]\n\t"
      "s_lshl2_add_u32 %[span], %[t1], %[span]\n\t"
      "v_lshl_add_u32 %[pfoff], %[pfoff], 4, %[t4]\n\t"
      "s_lshl_b32 %[t1], %[span], 5\n\t"
      "s_or_b32 %[t1], %[t1], %[par]\n\t"
      "v_cmp_gt_u32 vcc, %[t2], %[pfoff]\n\t"
      "v_and_or_b32 %[poff], %[poff], 24, %[t1]\n\t"
      "s_lshl_b32 %[t4], %[t2], 1\n\t"
      "v_mad_u32_u24 %[poff], %[poff], %[t2], %[pfoff]\n\t"
      "v_mov_b32 %[pfoff], 0x7ff00000\n\t"
      "s_lshl_b32 %[t1], %[t2], 2\n\t"
      "v_cndmask_b32 %[poff], %[pfoff], %[poff], vcc\n\t"
      "s_mul_i32 %[t2], %[t2], 6\n\t"
      "s_waitcnt lgkmcnt(0)\n\t"
      "buffer_load_dwordx4 %[p0], %[poff], %[rP], 0 offen nt\n\t"
      "buffer_load_dwordx4 %[p1], %[poff], %[rP], %[t4] offen nt\n\t"
      "buffer_load_dwordx4 %[p2], %[poff], %[rP], %[t1] offen nt\n\t"
      "buffer_load_dwordx4 %[p3], %[poff], %[rP], %[t2] offen nt\n"
      "B_%=:\n\t"
      "s_lshl3_add_u32 %[t3], %[h], %[t3]\n\t"
      "s_lshl_b32 %[t3], %[t3], 14\n\t"
      "s_sub_u32 %[t3], %[t3], 0x400\n\t"
      "v_lshl_add_u32 %[pfoff], %[tid], 4, %[t3]\n\t"
      "s_barrier\n"
      "E_%=:"
      : [rX] "=&s"(rX), [rP] "=&s"(rP), [rWO] "=&s"(rWO), [rWS] "=&s"(rWS), [t0] "=&s"(t0), [wv] "=&s"(wv), [h] "=&s"(h), [par] "=&s"(par), [span] "=&s"(span),
        [ps] "=&s"(ps), [poff] "=&v"(poff), [pfoff] "=&v"(pfoff), [cpf] "=&v"(cpf), [x0] "=&v"(x0), [x1] "=&v"(x1), [p0] "=&v"(p0), [p1] "=&v"(p1), [p2] "=&v"(p2), [p3] "=&v"(p3), [ep] "=&s"(ep),
        [pc] "=&s"(pc), [t1] "=&s"(t1), [t2] "=&s"(t2), [t3] "=&s"(t3), [t4] "=&s"(t4)
      : [ka] "s"(ka), [bid] "s"(bid), [tid] "v"(tid), [cl] "i"(CODE_LINES - 1), [lf] "i"(L_F)
      : "vcc", "scc", "memory");
  }
  asm("" : "+s"(rX)); asm("" : "+s"(rWO)); asm("" : "+s"(rWS)); asm("" : "+s"(wv)); asm("" : "+s"(h)); asm("" : "+s"(par)); asm("" : "+s"(span)); asm("" : "+s"(t0));
  static_assert(WS_EPOCH == 0 && 2 * FF == 2752 && OOB == 0x7ff00000u && A_WO == 7 && A_WS == 5 && A_WD == 8 && L_F + 256 <= L_END, "constants of the block above");
#define INDICES   \
  asm volatile("" : "+s"(bid), "+s"(wv)); asm volatile("" : "+v"(tid)); \
  const int lane = tid & 63, g = lane >> 4, c = lane & 15, xcd = bid & 7, j = bid >> 3; \
  const int hq = j >> 3, b = (j >> 2) & 1, s = j & 3, i16 = hq * 4 + s; \
  const int dcnt = i16 < 11 ? 12 : 8, dstart = xcd * 172 + 8 * i16 + 4 * min(i16, 11)

  if (wv == 0) {
    INDICES;
    const unsigned tstart = (unsigned)t0;
    if (lane < 9) LDSW(L_TB + lane * 4) = tstart - (1u << 30);
    FENCE();
    {
      float e[16];
      asm volatile("s_waitcnt vmcnt(0)" : "+v"(x0), "+v"(x1), "+v"(cpf) :: "memory");
      sfor<4>([&](auto ec) INL { constexpr int k = decltype(ec)::v; const unsigned u0 = x0[k], u1 = x1[k];
        e[2 * k] = bflo(u0); e[2 * k + 1] = bfhi(u0); e[8 + 2 * k] = bflo(u1); e[9 + 2 * k] = bfhi(u1); });
      float a = (((e[0] + e[1]) + (e[2] + e[3])) + ((e[4] + e[5]) + (e[6] + e[7]))) + (((e[8] + e[9]) + (e[10] + e[11])) + ((e[12] + e[13]) + (e[14] + e[15])));
      a = row_sum(a); a = sw16(a, a) * (1.f / 512.f);
      float v0 = 0.f, v1 = 0.f;
      sfor<8>([&](auto ec) INL { constexpr int k = decltype(ec)::v; const float d0 = e[k] - a, d1 = e[8 + k] - a; v0 = fmaf(d0, d0, v0); v1 = fmaf(d1, d1, v1); });
      float v = row_sum(v0 + v1); v = sw16(v, v) * (1.f / 512.f);
      const float mean = rbf(a), rden = __builtin_amdgcn_rcpf(rbf(__builtin_amdgcn_sqrtf(rbf(rbf(v) + 1e-5f))));
      u32x4 n0, n1;
      sfor<4>([&](auto ec) INL { constexpr int k = decltype(ec)::v;
        const unsigned d0 = pkbf(e[2 * k] - mean, e[2 * k + 1] - mean), d1 = pkbf(e[8 + 2 * k] - mean, e[9 + 2 * k] - mean);
        n0[k] = pkbf(bflo(d0) * rden, bfhi(d0) * rden); n1[k] = pkbf(bflo(d1) * rden, bfhi(d1) * rden); });
      *(u32x4*)(lds + L_XN + lane * 32) = n0; *(u32x4*)(lds + L_XN + lane * 32 + 16) = n1;
    }
    CLOB(); LDSW(F_XN) = 1u;
    FENCE();
    asm volatile("s_waitcnt lgkmcnt(0)" : "+s"(ep) :: "memory");
    const rsrc_t rV = ka[A_V], rOut = ka[A_OUT];
    unsigned ooff = (lane < 16 && s == 0) ? lane * 2 : OOB;
    const unsigned xr = __builtin_amdgcn_raw_buffer_load_b32(rX, ooff & ~2u, b * 1024 + h * 32, 0);
    const unsigned tl0 = __builtin_amdgcn_raw_buffer_load_b32(rV, 0, ((b * 32 + h) * 4 + s) << 18, NT), tl1 = __builtin_amdgcn_raw_buffer_load_b32(rOut, ooff & ~2u, b * 1024 + h * 32, 0);
    const unsigned tag = ((ep * 747796405u + 2891336453u) & ~0xfu) | ((ep + 1u) & 0xfu);
    LDSW(L_TAG) = tag;
    const int mat = bid >= 88;
    const unsigned dQ = WS_QX + (h * 8 + (j & 7)) * 1024, dF = WS_FX + (mat * 8 + xcd) * (2 * FROW * 4) + ((j - 11 * mat) * 128 + 32 - 32 * (xcd & 1)) * 4;
    unsigned fvo = bid < 176 ? (lane & 31) * 16 + (lane >> 5) * (FROW * 4) : OOB, qo = (lane >> 5) * 1024 + (lane & 31) * 16;
    const bool fon = 4 * (lane & 3) < dcnt;
    unsigned fo = fon ? (lane >> 2) * (2 * FROW * 4) + (lane & 3) * 16 : OOB, fmask = fon ? 0xffu : 0u;
    const unsigned fbase = WS_FX + b * (FROW * 4) + (32 + dstart) * 4;
    PIN(fvo); PIN(qo); PIN(fo); PIN(fmask);
    lds_wait(lds, F_PL, 8);
    { const unsigned char* src = lds + L_PL + lane * 16;
      const f32x4 vq = (*(const f32x4*)src + *(const f32x4*)(src + 1024)) + (*(const f32x4*)(src + 2048) + *(const f32x4*)(src + 3072));
      const f32x4 vf = (*(const f32x4*)(src + 4096) + *(const f32x4*)(src + 5120)) + (*(const f32x4*)(src + 6144) + *(const f32x4*)(src + 7168));
      __builtin_amdgcn_raw_buffer_store_b128(tagged4(vq, 3u, tag), rWS, lane * 16, dQ, SC1);
      __builtin_amdgcn_raw_buffer_store_b128(tagged4(vf, 0xffu, tag), rWS, fvo, dF, SC1);
    }
    asm volatile("s_waitcnt vmcnt(0)" ::: "memory");
    { u32x4 qp[4], fp; int spins = 0;
      _Pragma("unroll 1") do {
        CLOB();
        sfor<4>([&](auto kc) INL { constexpr int k = decltype(kc)::v; qp[k] = ld16(rWS, qo, WS_QX + h * 8192 + b * 512 + k * 2048, SC1); });
        fp = ld16(rWS, fo, fbase, SC1);
      } while (any((((mism(qp[0], tag) | mism(qp[1], tag) | mism(qp[2], tag) | mism(qp[3], tag)) & 3u) | (mism(fp, tag) & fmask)) != 0) && ++spins < SPIN_CAP);
      const f32x4 qs = (untag(qp[0], 3u) + untag(qp[1], 3u)) + (untag(qp[2], 3u) + untag(qp[3], 3u));
      *(u32x2*)(lds + L_Q + (lane & 31) * 8) = u32x2{pkbf(sw32(qs.x, qs.x), sw32(qs.y, qs.y)), pkbf(sw32(qs.z, qs.z), sw32(qs.w, qs.w))};
      const f32x4 f = untag(fp, 0xffu);
      float fa = sw16(f.x, f.y), fb = sw16(f.z, f.w);
      fa += dppf<ROR8>(fa); fb += dppf<ROR8>(fb); fa += dppf<ROR4>(fa); fb += dppf<ROR4>(fb);
      const auto gu = __builtin_amdgcn_permlane32_swap(__float_as_uint(fa), __float_as_uint(fb), false, false);
      const float gv = rbf(__uint_as_float(gu[0])), uv = rbf(__uint_as_float(gu[1]));
      const float z = 0.7978845608028654f * (gv + 0.044715f * gv * gv * gv);
      const float ge = rbf(gv * __builtin_amdgcn_rcpf(1.f + __builtin_amdgcn_exp2f(-2.f * LOG2E * z)));
      if (c < 4) *(u16*)(lds + L_H + (4 * c + g) * 2) = (u16)pkbf(ge * uv, 0.f);
      CLOB(); LDSW(F_Q) = 1u;
    }
    TOUCH(tl0); TOUCH(tl1); TOUCH(xr);
    { lds_wait(lds, F_KM, 8);
      const unsigned tK = tstart + ((LDSW(L_T2) - 8u * tstart) >> 3);
      __builtin_amdgcn_raw_buffer_store_b64(u32x2{tK, tag}, rWS, (lane == 0 && s == hq && b == (hq & 1)) ? 0u : OOB, WS_TM + h * 8, SC1);
      for (int k = 0; k < TDELAY; ++k) __builtin_amdgcn_s_sleep(16);
      const int dn = (int)(now() - tK);
      const u32x2 ta = __builtin_amdgcn_raw_buffer_load_b64(rWS, (lane & 31) * 8, WS_TM, SC1);
      const float mt = wave_sum((float)(ta.y == tag ? (int)(ta.x - tK) : dn)) * (1.f / 64.f);
      if (lane < 9) LDSW(L_TB + lane * 4) = tK + (int)fminf(mt, 400.f) + (lane ? ((2 * lane - 9) * STAGW) >> 5 : 0);
    }
    { const unsigned mb = WS_ML + (b * 32 + h) * 128;
      lds_wait(lds, F_ML, 8);
      const f32x2 ml = *(const f32x2*)(lds + L_ML + (lane & 7) * 8);
      float m = fmaxf(ml.x, dppf<XOR1>(ml.x)); m = fmaxf(m, dppf<XOR2>(m)); m = fmaxf(m, dppf<HMIR>(m));
      float l = ml.y * __builtin_amdgcn_exp2f((ml.x - m) * LOG2E); l += dppf<XOR1>(l); l += dppf<XOR2>(l); l += dppf<HMIR>(l);
      __builtin_amdgcn_raw_buffer_store_b128(u32x4{__float_as_uint(m), tag, __float_as_uint(l), tag}, rWS, lane == 0 ? 0u : OOB, mb + s * 16, SC1);
      __builtin_amdgcn_s_sleep(127);
      if (bid == 0) {
        int spins = 0;
        _Pragma("unroll 1") while (any(__builtin_amdgcn_raw_buffer_load_b32(rWS, (lane & 31) * 8 + 4, WS_TM, SC1) != tag) && ++spins < SPIN_CAP) { __builtin_amdgcn_s_sleep(8); CLOB(); }
        __builtin_amdgcn_raw_buffer_store_b32(tag, rWS, lane == 0 ? 0u : OOB, WS_EPOCH, SC1);
      }
      u32x4 mp; int spins = 0;
      _Pragma("unroll 1") do {
        __builtin_amdgcn_s_sleep(8); CLOB();
        mp = ld16(rWS, (lane & 3) * 16, mb, SC1);
      } while (any(mp.y != tag || mp.w != tag) && ++spins < SPIN_CAP);
      const float mj = __uint_as_float(mp.x), lj = __uint_as_float(mp.z);
      float M = fmaxf(mj, dppf<XOR1>(mj)); M = fmaxf(M, dppf<XOR2>(M));
      float L = lj * __builtin_amdgcn_exp2f((mj - M) * LOG2E); L += dppf<XOR1>(L); L += dppf<XOR2>(L);
      *(f32x2*)(lds + L_MR) = f32x2{M, __builtin_amdgcn_rcpf(L)};
      CLOB(); LDSW(F_MR) = 1u;
    }
    unsigned rrd = L_RL + c * 32; float xf = (lane & 1) ? bfhi(xr) : bflo(xr);
    PIN(rrd); PIN(xf); PIN(ooff);
    wg_barrier();
    if (s != 0) return;
    wg_barrier();
    const f32x4 r4 = *(const f32x4*)(lds + rrd) + *(const f32x4*)(lds + rrd + 16);
    __builtin_amdgcn_raw_buffer_store_b16((u16)pkbf(xf + ((r4.x + r4.y) + (r4.z + r4.w)), 0.f), rOut, ooff, b * 1024 + h * 32, 0);
    return;
  }

  u32x4 PF[2], KV[64];
  PF[0] = ld16(rWO, pfoff, 0, 0); PF[1] = ld16(rWO, pfoff, 8192, 0);
  CLOB(); FENCE();
  const rsrc_t rK = ka[A_K], rV = ka[A_V], rWD = ka[A_WD];
  INDICES;
  const int w = wv - 1, rot = (bid << 12) & 0x38000;
  unsigned kvoff = (((b * 32 + h) * 4 + s) << 18) + (tid - 64) * 16; PIN(kvoff);
  u32x4 sel, q;
  u32x2 WO[32], WD[4];
  u32x8 BO[8];
  unsigned wooff = 0, wdoff = 0, owr = 0, ord = 0, yst = 0, yld = 0, rwr = 0, ytag = 0, vzero = 0, vw4 = wv * 4;
  int sidx = 0;
  f32x4 oacc = {0.f, 0.f, 0.f, 0.f}, yacc[4];
  float t1 = 0.f, t2 = 0.f, t3 = 0.f, t4 = 0.f, sc[2] = {0.f, 0.f}, mw = 0.f, cw = 0.f;
  unsigned pp[2] = {0u, 0u};
  auto mkA = [&](unsigned src) INL -> u32x4 { u32x4 a; a.x = __builtin_amdgcn_perm(src, src, sel.x); a.y = __builtin_amdgcn_perm(src, src, sel.y);
                                              a.z = __builtin_amdgcn_perm(src, src, sel.z); a.w = __builtin_amdgcn_perm(src, src, sel.w); return a; };
  auto tile_off = [&](int t) INL { int r = rot; asm volatile("" : "+s"(r)); return (r + t * 8192) & 0x3ffff; };

  auto consumeK = [&](auto tc) INL {
    constexpr int t = decltype(tc)::v;
    const u32x4 k = KV[t];
    float d = dot2(k.x, q.x, 0.f); d = dot2(k.y, q.y, d); d = dot2(k.z, q.z, d); d = dot2(k.w, q.w, d);
    if constexpr ((t & 1) == 0) { t1 = d; PIN(t1); }
    else {
      { const float keep = (c & 8) ? d : t1, send = (c & 8) ? t1 : d; d = keep + dppf<ROR8>(send); }
      if constexpr ((t & 2) == 0) { t2 = d; PIN(t2); }
      else {
        { const float keep = (c & 4) ? d : t2, send = (c & 4) ? t2 : d; d = keep + dppf<HMIR>(send); }
        if constexpr ((t & 4) == 0) { t3 = d; PIN(t3); }
        else {
          { const float keep = (c & 2) ? d : t3, send = (c & 2) ? t3 : d; d = keep + dppf<XOR2>(send); }
          if constexpr ((t & 8) == 0) { t4 = d; PIN(t4); }
          else { const float keep = (c & 1) ? d : t4, send = (c & 1) ? t4 : d; sc[t >> 4] = keep + dppf<XOR1>(send); PIN(sc[t >> 4]); }
        }
      }
    }
    if constexpr (t == 31) {
      const float s0 = rbf(rbf(sc[0]) * 0.08838834764831845f), s1 = rbf(rbf(sc[1]) * 0.08838834764831845f);
      mw = wave_max(fmaxf(s0, s1));
      const float p0 = __builtin_amdgcn_exp2f((s0 - mw) * LOG2E), p1 = __builtin_amdgcn_exp2f((s1 - mw) * LOG2E);
      const float lw = wave_sum(p0 + p1);
      pp[0] = pkbf(p0, p0 - rbf(p0)); pp[1] = pkbf(p1, p1 - rbf(p1)); PIN(pp[0]); PIN(pp[1]);
      if (lane == 0) { *(f32x2*)(lds + L_ML + w * 8) = f32x2{mw, lw}; lds_add(lds, F_ML, 1u); }
    }
  };

  constexpr Sched S{};
  sfor<S.n>([&](auto nc) INL {
    constexpr Act A = S.a[decltype(nc)::v];
    constexpr int i = A.i;
    if constexpr (A.issue) {
      if constexpr (A.t == T_K || A.t == T_V) {
        constexpr int n = i + (A.t == T_V ? 32 : 0);
        if constexpr (n >= APACE0) { pace<(AEND - (63 - n) * ATAU) / 16>(vw4); FENCE(); }
        KV[n] = ld16(A.t == T_V ? rV : rK, kvoff, tile_off(i), NT);
      }
      else if constexpr (A.t == T_WO) WO[i] = __builtin_amdgcn_raw_buffer_load_b64(rWO, wooff, ((i >> 3) * 32 + (i & 7)) * 1024, NT);
      else if constexpr (A.t == T_WD) WD[i] = __builtin_amdgcn_raw_buffer_load_b64(rWD, wdoff, i * 1024, NT);
    } else {
      if constexpr (A.t == T_P) {
        if constexpr (i == 0) {
          const unsigned sb = (c & 8) ? 0x0302u : 0x0100u, one = (c & 1) ? ((sb << 16) | 0x0c0cu) : (0x0c0c0000u | sb); const int v = (c & 7) >> 1;
          sel.x = v == 0 ? one : 0x0c0c0c0cu; sel.y = v == 1 ? one : 0x0c0c0c0cu; sel.z = v == 2 ? one : 0x0c0c0c0cu; sel.w = v == 3 ? one : 0x0c0c0c0cu;
          wooff = h * 131072 + g * 8192 + w * 128 + c * 8;
          wdoff = (4 * g < dcnt) ? (unsigned)(dstart * 1024 + g * 4096 + w * 128 + c * 8) : OOB;
          owr = L_OL + (w + (lane >= 32 ? 8 : 0)) * OL_STR + c * 8 + (g & 1) * 4;
          ord = L_OL + c * OL_STR + g * 16;
          sidx = (c & 8) ? 0xeeee : 0x4444;
          yst = g ? OOB : WS_Y + (b * 128 + h * 4 + s) * 2048 + w * 256 + c * 16;
          yld = WS_Y + b * 128 * 2048 + h * 64 + ((tid - 64) >> 2) * 2048 + (lane & 3) * 16;
          rwr = c < 4 ? L_RL + (4 * c + g) * 32 + w * 4 : L_PL + lane * 4;
          PIN(sel); PIN(wooff); PIN(wdoff); PIN(owr); PIN(ord); PIN(sidx); PIN(yst); PIN(yld); PIN(rwr); PIN(vzero); PIN(vw4);
        }
        if constexpr (i < 3) asm volatile("s_waitcnt vmcnt(5)" ::: "memory");
        else asm volatile("s_waitcnt vmcnt(5)" : "+v"(p0), "+v"(p1), "+v"(p2), "+v"(p3) :: "memory");
        if constexpr (i == 3) {
          lds_wait(lds, F_XN, 1u);
          const u32x4 xa = *(const u32x4*)(lds + L_XN + (c >> 3) * 1024 + g * 16 + span * 64);
          f32x4 pacc = {0.f, 0.f, 0.f, 0.f};
          const u32x4 P[4] = {p0, p1, p2, p3};
          sfor<4>([&](auto ec) INL { constexpr int e = decltype(ec)::v; const unsigned u = xa[e];
            pacc = mfma32(mkA(__builtin_amdgcn_perm(u, u, par ? 0x03020302u : 0x01000100u)), P[e], pacc); });
          *(f32x4*)(lds + L_PL + w * 1024 + (g >> 1) * 512 + c * 32 + (g & 1) * 16) = pacc;
          if (lane == 0) lds_add(lds, F_PL, 1u);
        }
      } else if constexpr (A.t == T_PF) { TOUCH(PF[i]);
      } else if constexpr (A.t == T_K) {
        TOUCH(KV[i]);
        if constexpr (i == KM) { const unsigned t = now(); if (lane == 0) { lds_add(lds, L_T2, t); lds_add(lds, F_KM, 1u); } }
        if constexpr (i == QPOS) {
          lds_wait(lds, F_Q, 1u);
          q = *(const u32x4*)(lds + L_Q + c * 16);
          ytag = LDSW(L_TAG) & 0xfu;
          PIN(ytag);
        }
      } else if constexpr (A.t == T_WD) {
        TOUCH(WD[i]);
        if constexpr (i == 3) {
          u32x2 hA = *(const u32x2*)(lds + L_H + g * 8);
          if (c != 0) hA = u32x2{0u, 0u};
          sfor<4>([&](auto tc) INL { constexpr int t = decltype(tc)::v; constexpr unsigned ps = (t & 1) ? 0x07060302u : 0x05040100u;
            const unsigned w0 = WD[0][t >> 1], w1 = WD[1][t >> 1], w2 = WD[2][t >> 1], w3 = WD[3][t >> 1];
            const u32x2 bb = {__builtin_amdgcn_perm(w1, w0, ps), __builtin_amdgcn_perm(w3, w2, ps)};
            yacc[t] = mfma16(hA, bb, f32x4{0.f, 0.f, 0.f, 0.f}); PIN(yacc[t]); });
        }
      } else if constexpr (A.t == T_WO) {
        TOUCH(WO[i]);
        if constexpr ((i & 7) == 7) {
          constexpr int u = i >> 3;
          sfor<4>([&](auto tc) INL { constexpr int t = decltype(tc)::v; constexpr unsigned ps = (t & 1) ? 0x07060302u : 0x05040100u;
            sfor<4>([&](auto vc) INL { constexpr int v = decltype(vc)::v;
              const unsigned lo = WO[u * 8 + 2 * v][t >> 1], hi = WO[u * 8 + 2 * v + 1][t >> 1];
              BO[(u >> 1) * 4 + t][(u & 1) * 4 + v] = __builtin_amdgcn_perm(hi, lo, ps); });
            PIN(BO[(u >> 1) * 4 + t]); });
        }
      } else if constexpr (A.t == T_V) {
        oacc = mfma32(mkA(dppu<BCAST + brev4(i & 15)>(pp[i >> 4])), KV[32 + i], oacc); PIN(oacc);
        if constexpr (i == 27) {
          lds_wait(lds, F_MR, 1u);
          const f32x2 mr = *(const f32x2*)(lds + L_MR);
          cw = __builtin_amdgcn_exp2f((mw - mr.x) * LOG2E) * mr.y;
          PIN(cw);
        }
      }
      sfor<A.k1 - A.k0>([&](auto kc) INL { FENCE(); consumeK(IC<A.k0 + decltype(kc)::v>{}); });
    }
    FENCE();
  });

  *(unsigned*)(lds + owr) = pkbf(sw32(oacc.x, oacc.z) * cw, sw32(oacc.y, oacc.w) * cw);
  wg_barrier();
  { const u32x4 a0 = *(const u32x4*)(lds + ord), a1 = *(const u32x4*)(lds + ord + 64);
    FENCE();
    auto sum4 = [&](f32x4 u) INL { const f32x2 p = f32x2{u.x, u.y} + f32x2{u.z, u.w}; return p.x + p.y; };
    auto fin = [&](float y) INL { y = sw16(y, y); const unsigned t = ((__float_as_uint(y) + 8u) & ~0xfu) | ytag; return __builtin_amdgcn_permlane32_swap(t, t, false, false); };
    yacc[0] = smfma64(a0, BO[0], yacc[0], sidx); yacc[2] = smfma64(a0, BO[2], yacc[2], sidx); FENCE();
    yacc[0] = smfma64(a1, BO[4], yacc[0], sidx); yacc[2] = smfma64(a1, BO[6], yacc[2], sidx); FENCE();
    yacc[1] = smfma64(a0, BO[1], yacc[1], sidx); FENCE();
    float z0 = sum4(yacc[0]); PIN(z0); FENCE();
    yacc[3] = smfma64(a0, BO[3], yacc[3], sidx); FENCE();
    float z2 = sum4(yacc[2]); PIN(z2); FENCE();
    yacc[1] = smfma64(a1, BO[5], yacc[1], sidx); FENCE();
    float ya = sw32(z0, z2); PIN(ya); FENCE();
    yacc[3] = smfma64(a1, BO[7], yacc[3], sidx); FENCE();
    const auto sa = fin(ya);
    const auto sb = fin(sw32(sum4(yacc[1]), sum4(yacc[3])));
    __builtin_amdgcn_raw_buffer_store_b128(u32x4{sa[0], sb[0], sa[1], sb[1]}, rWS, yst, 0, SC1);
  }
  if (s != 0) return;
  { u32x4 y; int spins = SPIN_CAP;
    pace<AEND / 16 + YPOLL>(vzero);
    unsigned t0_, t1_;
    asm volatile("s_waitcnt vmcnt(0)\n"
                 "L_%=:\n\tbuffer_load_dwordx4 v[2:5], %[off], %[rs], 0 offen sc1\n\ts_waitcnt vmcnt(0)\n\t"
                 "v_bitop3_b32 %[t0], v2, v3, %[tag] bitop3:0x7e\n\tv_bitop3_b32 %[t1], v4, v5, %[tag] bitop3:0x7e\n\t"
                 "v_bitop3_b32 %[t0], %[t0], %[t1], 15 bitop3:0xa8\n\tv_cmp_ne_u32 vcc, 0, %[t0]\n\ts_cbranch_vccz D_%=\n\t"
                 "s_sub_u32 %[sp], %[sp], 1\n\ts_cbranch_scc0 L_%=\n"
                 "D_%=:"
                 : "=&{v[2:5]}"(y), [t0] "=&v"(t0_), [t1] "=&v"(t1_), [sp] "+s"(spins) : [off] "v"(yld), [rs] "s"(rWS), [tag] "v"(ytag) : "vcc", "scc", "memory");
    float v = tr4(untag(y, 0xfu)); v += dppf<ROR8>(v); v += dppf<ROR4>(v);
    *(float*)(lds + rwr) = v;
    wg_barrier();
  }
}
}

extern "C" void kernel_launch(void* const* d_in, const int* in_sizes, int n_in, void* d_out, int out_size, void* d_ws, size_t ws_size, hipStream_t stream) {
  const unsigned F = 0x00020000u;
  fused_decode<<<256, 576, 0, stream>>>(d_in[0], 2048u, F, d_in[3], 512u * 8192u, F, d_in[6], 512u * 2u * FF, F, d_in[7], 512u * 2u * FF, F, d_in[1], 64u << 20, F,
                                        d_ws, WS_END, F, d_in[2], 64u << 20, F, d_in[4], 4096u * 1024u, F, d_in[8], FF * 1024u, F, d_out, 2048u, F);
}
```
